```python
import torch

def _softmax(x, axis=-1):
    return torch.softmax(x, dim=axis)
def _gelu(x):
    return torch.nn.functional.gelu(x, approximate="tanh")
def _silu(x):
    return torch.nn.functional.silu(x)
def _sigmoid(x):
    return torch.sigmoid(x)
def _layernorm(x, axis=-1, eps=1e-5):
    m = x.mean(dim=axis, keepdim=True)
    v = x.var(dim=axis, keepdim=True, unbiased=False)
    return (x - m) / torch.sqrt(v + eps)
def _scatter_add(tgt, idx, src, axis=0):
    return tgt.clone().index_add_(axis, idx, src)
def _segment_sum(src, seg, n):
    out = torch.zeros((n,) + src.shape[1:], dtype=src.dtype)
    return out.index_add_(0, seg, src)
def _pad(x, pw):
    flat = tuple(v for pair in reversed(pw) for v in pair)
    return torch.nn.functional.pad(x, flat)

kernel_name = 'parallel_sum__sliding_local_dilated__dense_activation_gelu__prompt'

def setup_inputs():

    _w = (lambda a: (lambda t: t.to(torch.bfloat16) if t.is_floating_point() else t)(torch.as_tensor(a)))
    def _A_setup():
        B, S, D = 1, 2048, 512
        return {
            "x": _w((lambda *s: torch.randn(*s, dtype=torch.bfloat16) * 0.05)(B, S, D)),
            "wqkv": _w((lambda *s: torch.randn(*s, dtype=torch.bfloat16) * 0.05)(D, 3 * D)),
            "wo": _w((lambda *s: torch.randn(*s, dtype=torch.bfloat16) * 0.05)(D, D)),
        }
    _A_in = _A_setup()

    def _B_setup():
        B, S, D, F = 1, 2048, 512, 1376
        return {
            "x": _w((lambda *s: torch.randn(*s, dtype=torch.bfloat16) * 0.05)(B, S, D)),
            "w1": _w((lambda *s: torch.randn(*s, dtype=torch.bfloat16) * 0.8)(D, F) * (D ** -0.5)),
            "b1": _w((lambda *s: torch.randn(*s, dtype=torch.bfloat16) * 0.8)(F) * 0.01),
            "w2": _w((lambda *s: torch.randn(*s, dtype=torch.bfloat16) * 0.8)(F, D) * (F ** -0.5)),
            "b2": _w((lambda *s: torch.randn(*s, dtype=torch.bfloat16) * 0.8)(D) * 0.01),
        }
    _B_in = _B_setup()

    return {
        "A_x": _A_in["x"],
        "A_wqkv": _A_in["wqkv"],
        "A_wo": _A_in["wo"],
        "B_x": _B_in["x"],
        "B_w1": _B_in["w1"],
        "B_b1": _B_in["b1"],
        "B_w2": _B_in["w2"],
        "B_b2": _B_in["b2"],
    }

def reference(A_x, A_wqkv, A_wo, B_x, B_w1, B_b1, B_w2, B_b2):
    def _A_ref(x, wqkv, wo):
        B, S, D = x.shape
        H = 32; Dh = D // H
        W, dil = 8, 2
        qkv = (lambda x, *d: x.permute(*d))((x @ wqkv).reshape(B, S, 3, H, Dh), 2, 0, 3, 1, 4)
        q, k, v = qkv[0], qkv[1], qkv[2]
        scale = Dh ** -0.5
        pos = torch.arange(S)
        off = torch.arange(W) * dil
        idx = pos[:, None] - off[None, :]
        mask = idx >= 0
        idx_c = torch.where(mask, idx, 0).reshape(-1)
        kg = k[:, :, idx_c, :].reshape(B, H, S, W, Dh)
        vg = v[:, :, idx_c, :].reshape(B, H, S, W, Dh)
        logits = torch.einsum("bhsd,bhswd->bhsw", q, kg) * scale
        logits = torch.where(mask[None, None, :, :], logits, -1e9)
        attn = _softmax(logits, axis=-1)
        o = torch.einsum("bhsw,bhswd->bhsd", attn, vg)
        return (lambda x, *d: x.permute(*d))(o, 0, 2, 1, 3).reshape(B, S, D) @ wo

    def _B_ref(x, w1, b1, w2, b2):
        h = torch.einsum("bsd,df->bsf", x, w1) + b1
        h = _gelu(h)
        return torch.einsum("bsf,fd->bsd", h, w2) + b2

    x = A_x
    xn = _layernorm(x, axis=-1)
    return x + _A_ref(xn, A_wqkv, A_wo) + _B_ref(xn, B_w1, B_b1, B_w2, B_b2)
```

```cpp
#include <hip/hip_runtime.h>
#include <hip/hip_bf16.h>
typedef __hip_bfloat16 bf16;
__device__ inline float b2f(bf16 v){ return __bfloat162float(v);}
__device__ inline bf16 f2b(float v){ return __float2bfloat16(v);}
constexpr int S=2048,D=512,F=1376,H=32,DH=16;
__global__ void k_ln(const bf16* x, float* xn){
  int r=blockIdx.x; int t=threadIdx.x;
  float v[8]; float s=0; for(int i=0;i<8;i++){v[i]=b2f(x[r*D+t*8+i]); s+=v[i];}
  for(int m=32;m>=1;m>>=1) s+=__shfl_xor(s,m);
  float mean=s/D; float q=0; for(int i=0;i<8;i++){float d=v[i]-mean; q+=d*d;}
  for(int m=32;m>=1;m>>=1) q+=__shfl_xor(q,m);
  float rs=rsqrtf(q/D+1e-5f);
  for(int i=0;i<8;i++) xn[r*D+t*8+i]=(v[i]-mean)*rs;
}
__global__ void k_gemm(const float* A,int lda,const bf16* B,int ldb,float* C,int ldc,int K,int N){
  int n=blockIdx.x*256+threadIdx.x; int m0=blockIdx.y*8; if(n>=N) return;
  float acc[8]={};
  for(int k=0;k<K;k++){ float b=b2f(B[k*ldb+n]); for(int i=0;i<8;i++) acc[i]+=A[(m0+i)*lda+k]*b; }
  for(int i=0;i<8;i++) C[(m0+i)*ldc+n]=acc[i];
}
__global__ void k_attn(const float* qkv,float* act){
  int s=blockIdx.x; int h=threadIdx.x;
  float lg[8]; float mx=-1e30f;
  for(int w=0;w<8;w++){ int t=s-2*w; float d=-1e9f; if(t>=0){ d=0; for(int i=0;i<16;i++) d+=qkv[s*1536+h*16+i]*qkv[t*1536+512+h*16+i]; d*=0.25f;} lg[w]=d; mx=fmaxf(mx,d);}
  float sum=0; for(int w=0;w<8;w++){ lg[w]=expf(lg[w]-mx); sum+=lg[w]; }
  for(int i=0;i<16;i++){ float o=0; for(int w=0;w<8;w++){ int t=s-2*w; if(t<0)t=0; o+=lg[w]*qkv[t*1536+1024+h*16+i]; } act[s*1888+h*16+i]=o/sum; }
}
__global__ void k_gelu(const float* hpre,const bf16* b1,float* act){
  int i=blockIdx.x*256+threadIdx.x; int s=i/F,f=i%F; if(s>=S) return;
  float v=hpre[s*F+f]+b2f(b1[f]); float u=0.7978845608028654f*(v+0.044715f*v*v*v); act[s*1888+512+f]=0.5f*v*(1.f+tanhf(u));
}
__global__ void k_fin(const bf16* x,const float* a,const float* b,const bf16* b2,bf16* out){
  int i=blockIdx.x*256+threadIdx.x; out[i]=f2b(b2f(x[i])+a[i]+b[i]+b2f(b2[i%D]));
}
extern "C" void kernel_launch(void* const* d_in, const int* in_sizes, int n_in, void* d_out, int out_size, void* d_ws, size_t ws_size, hipStream_t stream) {
  const bf16* x=(const bf16*)d_in[0]; const bf16* wqkv=(const bf16*)d_in[1]; const bf16* wo=(const bf16*)d_in[2];
  const bf16* w1=(const bf16*)d_in[4]; const bf16* b1=(const bf16*)d_in[5]; const bf16* w2=(const bf16*)d_in[6]; const bf16* b2=(const bf16*)d_in[7];
  float* ws=(float*)d_ws; float* xn=ws; float* qkv=xn+S*D; float* hpre=qkv+S*1536; float* act=hpre+S*F; float* oa=act+S*1888; float* ob=oa+S*D;
  k_ln<<<S,64,0,stream>>>(x,xn);
  k_gemm<<<dim3(6,S/8),256,0,stream>>>(xn,D,wqkv,1536,qkv,1536,D,1536);
  k_gemm<<<dim3(6,S/8),256,0,stream>>>(xn,D,w1,F,hpre,F,D,F);
  k_attn<<<S,32,0,stream>>>(qkv,act);
  k_gelu<<<(S*F+255)/256,256,0,stream>>>(hpre,b1,act);
  k_gemm<<<dim3(2,S/8),256,0,stream>>>(act,1888,wo,D,oa,D,D,D);
  k_gemm<<<dim3(2,S/8),256,0,stream>>>(act+512,1888,w2,D,ob,D,F,D);
  k_fin<<<S*D/256,256,0,stream>>>(x,oa,ob,b2,(bf16*)d_out);
}
```

```cpp
#include <hip/hip_runtime.h>
#include <stdint.h>

typedef __attribute__((ext_vector_type(8))) __bf16 bf16x8;
typedef __attribute__((ext_vector_type(2))) __bf16 bf16x2;
typedef __attribute__((ext_vector_type(4))) short s16x4;
typedef __attribute__((ext_vector_type(4))) float f32x4;
typedef __attribute__((ext_vector_type(2))) float f32x2;
typedef __attribute__((ext_vector_type(16))) float f32x16;
typedef __attribute__((ext_vector_type(4))) unsigned u32x4;
typedef __attribute__((ext_vector_type(2))) unsigned u32x2;
#define LAS __attribute__((address_space(3)))
#define GAS __attribute__((address_space(1)))
typedef unsigned long long u64;

#define DIAG 0
#define ABL_NS 0
#define ROT1_MODE 1
#define ROT2_MODE 1

constexpr int NK16 = 118;
constexpr int SLOT = 43008, XB = 18432;
constexpr int ST_MEAN = 129024, ST_RSTD = ST_MEAN + 576, ST_CS = ST_MEAN + 1152;
constexpr int KVX = 19 * 8192;
constexpr int WS_FLAG = 1024, WS_DIAG = 32768, WS_ACT = 65536;
constexpr int ACT_BYTES = 64 * NK16 * 1024;
constexpr int NSTAMP = 9;
constexpr unsigned SPIN_CAP = 1u << 20;

struct Params {
  const char* x; const char* wqkv; const char* wo; const char* w1; const char* b1; const char* w2; const char* b2;
  char* out; char* ws;
};

__device__ __forceinline__ void glds16(unsigned voff, const char* sbase, unsigned lds) {
  asm volatile("s_mov_b32 m0, %2\n\ts_nop 0\n\tglobal_load_lds_dwordx4 %0, %1" :: "v"(voff), "s"(sbase), "s"(lds) : "memory");
}
__device__ __forceinline__ void glds16_lo32(unsigned voff, const char* sbase, unsigned lds) {
  asm volatile("s_mov_b32 m0, %2\n\ts_mov_b32 exec_hi, 0\n\tglobal_load_lds_dwordx4 %0, %1\n\ts_mov_b32 exec_hi, -1" :: "v"(voff), "s"(sbase), "s"(lds) : "memory");
}
template <int N> __device__ __forceinline__ void wait_vm() { asm volatile("s_waitcnt vmcnt(%0) lgkmcnt(0)" :: "i"(N) : "memory"); }
__device__ __forceinline__ void wait_lgkm0() { asm volatile("s_waitcnt lgkmcnt(0)" ::: "memory"); }
__device__ __forceinline__ void bar() { asm volatile("s_barrier" ::: "memory"); }
__device__ __forceinline__ unsigned pk_bf16(float a, float b) { f32x2 p = {a, b}; return __builtin_bit_cast(unsigned, __builtin_convertvector(p, bf16x2)); }
__device__ __forceinline__ float dot2(unsigned a, unsigned b, float c) { return __builtin_amdgcn_fdot2_f32_bf16(__builtin_bit_cast(bf16x2, a), __builtin_bit_cast(bf16x2, b), c, false); }
__device__ __forceinline__ u32x2 trd(const LAS char* p) { return __builtin_bit_cast(u32x2, __builtin_amdgcn_ds_read_tr16_b64_v4i16((LAS s16x4*)p)); }
__device__ __forceinline__ bf16x8 b8(u32x4 v) { return __builtin_bit_cast(bf16x8, v); }
__device__ __forceinline__ float bf_lo(unsigned v) { return __uint_as_float(v << 16); }
__device__ __forceinline__ float bf_hi(unsigned v) { return __uint_as_float(v & 0xffff0000u); }
__device__ __forceinline__ float xmax16_32(float v) {
  auto a = __builtin_amdgcn_permlane16_swap(__float_as_uint(v), __float_as_uint(v), false, false);
  v = fmaxf(__uint_as_float(a[0]), __uint_as_float(a[1]));
  auto b = __builtin_amdgcn_permlane32_swap(__float_as_uint(v), __float_as_uint(v), false, false);
  return fmaxf(__uint_as_float(b[0]), __uint_as_float(b[1]));
}
__device__ __forceinline__ u64 mix64(u64 z) {
  z += 0x9E3779B97F4A7C15ull; z = (z ^ (z >> 30)) * 0xBF58476D1CE4E5B9ull; z = (z ^ (z >> 27)) * 0x94D049BB133111EBull; return z ^ (z >> 31);
}
#define STAMP(i) do { if (DIAG) { asm volatile("s_memrealtime %0\n\ts_waitcnt lgkmcnt(0)" : "=s"(stamps[i]) :: "memory"); } } while (0)

__device__ __forceinline__ constexpr int b2grp(int s) {
  return s < 3 ? 16 + s : s < 7 ? 8 + s : s < 18 ? s - 7 : s < 21 ? s - 2 : s < 25 ? s - 10 : s < 28 ? s - 25 : s == 28 ? 15 : 19;
}

struct B2Ctx { const char* wo; const char* w2; int kq0, nq, rot, hf, q; unsigned voff, ldsb; };
__device__ __forceinline__ int phys_k16(const B2Ctx& c, int s) { int t = s + c.rot; t = t >= c.nq ? t - c.nq : t; t = (s >= c.nq) ? 0 : t; return c.kq0 + t; }
__device__ __forceinline__ void b2_issue(const B2Ctx& c, int s) {
  int row0 = phys_k16(c, s) * 16 + c.hf * 8;
  const char* src = row0 < 512 ? c.wo + row0 * 1024 : c.w2 + (row0 - 512) * 1024;
  glds16(c.voff, src, c.ldsb + b2grp(s) * 8192 + c.q * 2048 + c.hf * 1024);
}

template <bool H0, bool QKV>
__device__ __forceinline__ void phase1(const Params& P, LAS char* smem, unsigned ldsb, int p, int j, int c, int lane, int rot, const B2Ctx& b2c,
                                       f32x4 (&acc)[5][3], u64 (&stamps)[NSTAMP]) {
  constexpr int NRB = H0 ? 5 : 4;
  constexpr int NL = H0 ? (QKV ? 5 : 4) : 6;
  const int g = lane >> 4, m = lane & 15;
  unsigned vo[6];
  const char* base0; const char* baseh = nullptr; int kstride;
  if (H0) {
#pragma unroll
    for (int i = 0; i < 4; i++) vo[i] = (32 * c + 8 * i + (lane >> 3)) * 1024 + (((lane & 7) ^ (4 * (i & 1) + (lane >> 4))) * 16);
    { int row = 4 * c + ((lane >> 3) & 3); vo[4] = row * 1024 + (((lane & 7) ^ (row >> 1)) * 16); }
    vo[5] = 0;
    base0 = P.x + p * 131072; baseh = P.x + (p > 0 ? p * 131072 - 16384 : 0); kstride = 128;
  } else {
    const int nval = (j == 7) ? 18 : 22;
#pragma unroll
    for (int i = 0; i < 6; i++) {
      int u = (c * 6 + i) * 64 + lane, row = u / 24, ch = u - row * 24, sec = ch >> 3;
      int cc = (ch & 7) ^ (2 * (((row >> 1) & 1) | ((row >> 2) & 2)));
      if (QKV) vo[i] = row * 3072 + sec * 1024 + cc * 16;
      else { int c16 = sec * 8 + cc; c16 = c16 < nval ? c16 : nval - 1; vo[i] = row * 2752 + c16 * 16; }
    }
    base0 = QKV ? P.wqkv + j * 128 : P.w1 + j * 352; kstride = QKV ? 64 * 3072 : 64 * 2752;
  }
  auto issue = [&](int ch, int slot) {
    int kc = (ch + rot) & 7;
    const char* sb = base0 + kc * kstride;
    unsigned lb = ldsb + slot * SLOT;
    if (H0) {
#pragma unroll
      for (int i = 0; i < 4; i++) glds16(vo[i], sb, lb + (16 + 32 * c + 8 * i) * 128);
      if (QKV) glds16_lo32(vo[4], baseh + kc * 128, lb + 4 * c * 128);
    } else {
#pragma unroll
      for (int i = 0; i < 6; i++) glds16(vo[i], sb, lb + XB + (c * 6 + i) * 1024);
    }
  };
  issue(0, 0); issue(1, 1); issue(2, 2);
  if (H0 && !QKV) { if (c == 0) { int l = lane < (j == 7 ? 18 : 22) ? lane : 0; glds16(l * 16, P.b1 + j * 352, ldsb + KVX); } }
  const int a0 = (H0 ? 0 : 5 * 2048) + m * 128 + ((g ^ (m >> 1)) * 16);
  const int q4 = m >> 2, pp = lane & 3;
  const int b0 = XB + (8 * g + q4) * 384 + (((2 * c + (pp >> 1)) ^ (2 * ((q4 >> 1) | ((g & 1) << 1)))) * 16) + (pp & 1) * 8;
  const int w = (H0 ? 0 : 4) + c;
  const int s0 = (16 + 16 * w) * 128 + lane * 16;
  const int sh = 2 * w * 128 + (lane & 15) * 16;
  const bool ones = QKV ? (m < 2) : true;
  float sm[3] = {0.f, 0.f, 0.f}, sq[3] = {0.f, 0.f, 0.f};
#pragma unroll
  for (int r = 0; r < 5; r++)
#pragma unroll
    for (int s = 0; s < 3; s++) acc[r][s] = (f32x4){0.f, 0.f, 0.f, 0.f};
  int slot = 0;
  for (int ch = 0; ch < 8; ch++) {
    if (ch == 0) wait_vm<2 * NL>(); else if (ch < 6) wait_vm<NL>(); else if (ch == 6) wait_vm<NL + 3>(); else wait_vm<7>();
    bar();
    if (ch == 0) STAMP(1);
    if (ch >= 1 && ch <= 5) issue(ch + 2, slot == 0 ? 2 : slot - 1);
    if (ch == 5) { b2_issue(b2c, 0); b2_issue(b2c, 1); b2_issue(b2c, 2); }
    if (ch == 6) { b2_issue(b2c, 3); b2_issue(b2c, 4); b2_issue(b2c, 5); b2_issue(b2c, 6); }
    if (ch == 7) { b2_issue(b2c, 7); b2_issue(b2c, 8); b2_issue(b2c, 9); b2_issue(b2c, 10); b2_issue(b2c, 11); }
    LAS char* sb = smem + slot * SLOT;
#pragma unroll
    for (int ks = 0; ks < 2; ks++) {
      u32x4 B[3], A[5];
#pragma unroll
      for (int s = 0; s < 3; s++) {
        u32x2 lo = trd(sb + b0 + (32 * ks) * 384 + s * 128), hi = trd(sb + b0 + (32 * ks + 4) * 384 + s * 128);
        B[s] = (u32x4){lo.x, lo.y, hi.x, hi.y};
      }
#pragma unroll
      for (int r = 0; r < NRB; r++) {
        if (H0 && !QKV && r == 0) A[r] = (u32x4){0x3f803f80u, 0x3f803f80u, 0x3f803f80u, 0x3f803f80u};
        else A[r] = *(const LAS u32x4*)(sb + (a0 ^ (ks * 64)) + r * 2048);
      }
      if (H0 && QKV) {
#pragma unroll
        for (int e = 0; e < 4; e++) A[0][e] = ones ? 0x3f803f80u : A[0][e];
      }
      u32x4 T = *(const LAS u32x4*)(sb + s0 + ks * 1024);
#pragma unroll
      for (int e = 0; e < 4; e++) { sm[ks] = dot2(T[e], 0x3f803f80u, sm[ks]); sq[ks] = dot2(T[e], T[e], sq[ks]); }
      if (QKV && ks == 0 && !ABL_NS) {
        u32x4 Hh = *(const LAS u32x4*)(sb + sh);
#pragma unroll
        for (int e = 0; e < 4; e++) { sm[2] = dot2(Hh[e], 0x3f803f80u, sm[2]); sq[2] = dot2(Hh[e], Hh[e], sq[2]); }
      }
#pragma unroll
      for (int r = 0; r < NRB; r++)
#pragma unroll
        for (int s = 0; s < 3; s++) {
          if (QKV && s == 2) acc[r][s] = __builtin_amdgcn_mfma_f32_16x16x32_bf16(b8(A[r]), b8(B[s]), acc[r][s], 0, 0, 0);
          else acc[r][s] = __builtin_amdgcn_mfma_f32_16x16x32_bf16(b8(B[s]), b8(A[r]), acc[r][s], 0, 0, 0);
        }
    }
    slot = slot == 2 ? 0 : slot + 1;
  }
  STAMP(2);
#pragma unroll
  for (int i = 0; i < (QKV ? 3 : 2); i++) {
    float a = sm[i], b = sq[i];
#pragma unroll
    for (int d = 1; d < 8; d <<= 1) { a += __shfl_xor(a, d); b += __shfl_xor(b, d); }
    float mean = a * (1.f / 512.f), var = fmaxf(b * (1.f / 512.f) - mean * mean, 0.f), rs = __builtin_amdgcn_rsqf(var + 1e-5f);
    int row = i < 2 ? 16 + 16 * w + 8 * i + (lane >> 3) : 2 * w + ((lane >> 3) & 1);
    if ((lane & 7) == 0) { *(LAS float*)(smem + ST_MEAN + row * 4) = mean; *(LAS float*)(smem + ST_RSTD + row * 4) = rs; }
  }
  if (H0) {
    if (m == 0) {
      *(LAS f32x4*)(smem + ST_CS + (0 * 64 + c * 16 + 4 * g) * 4) = acc[0][0];
      *(LAS f32x4*)(smem + ST_CS + (1 * 64 + c * 16 + 4 * g) * 4) = acc[0][1];
      if (!QKV) *(LAS f32x4*)(smem + ST_CS + (2 * 64 + c * 16 + 4 * g) * 4) = acc[0][2];
    }
    if (QKV) {
      if (g == 0) *(LAS float*)(smem + ST_CS + (2 * 64 + c * 16 + m) * 4) = acc[0][2][0];
      *(LAS f32x4*)(smem + KVX + c * 2048 + lane * 16) = acc[4][1];
      *(LAS f32x4*)(smem + KVX + c * 2048 + 1024 + lane * 16) = acc[4][2];
    }
  } else {
#pragma unroll
    for (int r = 4; r >= 1; r--)
#pragma unroll
      for (int s = 0; s < 3; s++) acc[r][s] = acc[r - 1][s];
  }
  wait_lgkm0();
  bar();
  if (!H0 && QKV) {
    acc[0][1] = *(const LAS f32x4*)(smem + KVX + c * 2048 + lane * 16);
    acc[0][2] = *(const LAS f32x4*)(smem + KVX + c * 2048 + 1024 + lane * 16);
  }
}

__global__ __launch_bounds__(512, 2) void fused_block(Params P) {
  __shared__ __attribute__((aligned(16))) char smem_[163840];
  LAS char* smem = (LAS char*)smem_;
  const unsigned ldsb = (unsigned)(size_t)smem;
  u64 stamps[NSTAMP];
  STAMP(0);
  const int tid = threadIdx.x, lane = tid & 63, wave = __builtin_amdgcn_readfirstlane(tid >> 6);
  const int hf = wave >> 2, c = wave & 3, g = lane >> 4, m = lane & 15;
  const int b = blockIdx.x, xcd = b & 7, r5 = b >> 3;
  const int p = (xcd & 1) * 8 + (r5 & 7), jj = r5 >> 3, j = (xcd >> 1) * 2 + (jj & 1);
  const bool qkv = jj < 2;
  const int rot1 = ROT1_MODE == 0 ? 0 : ROT1_MODE == 1 ? (p + jj) : ROT1_MODE == 2 ? ((p + jj) & 1) * 4 : ROT1_MODE == 4 ? ((p + jj) & 3) * 2 : (p + 2 * jj);
  const int rt = xcd * 4 + jj, ct = r5 & 7;

  B2Ctx b2c;
  b2c.wo = P.wo + ct * 128; b2c.w2 = P.w2 + ct * 128; b2c.hf = hf; b2c.q = c; b2c.ldsb = ldsb;
  b2c.kq0 = c < 3 ? 30 * c : 89; b2c.nq = c < 2 ? 30 : 29;
  b2c.rot = ROT2_MODE == 0 ? 0 : ROT2_MODE == 1 ? ((ct + rt) & 1) * 15 : ROT2_MODE == 3 ? ((ct + rt) % 3) * 10 : ((ct + rt) & 3) * 7;
  b2c.voff = (lane >> 3) * 1024 + (((lane & 7) ^ (((lane >> 4) & 1) << 2)) * 16);

  f32x4 acc[5][3];
  if (qkv) { if (hf == 0) phase1<true, true>(P, smem, ldsb, p, j, c, lane, rot1, b2c, acc, stamps); else phase1<false, true>(P, smem, ldsb, p, j, c, lane, rot1, b2c, acc, stamps); }
  else     { if (hf == 0) phase1<true, false>(P, smem, ldsb, p, j, c, lane, rot1, b2c, acc, stamps); else phase1<false, false>(P, smem, ldsb, p, j, c, lane, rot1, b2c, acc, stamps); }
  STAMP(3);
  u64 e_old;
  asm volatile("s_load_dwordx2 %0, %1, 0x0" : "=s"(e_old) : "s"(P.ws) : "memory");
#pragma unroll
  for (int s = 12; s < 18; s++) b2_issue(b2c, s);

  const __amdgpu_buffer_rsrc_t act = __builtin_amdgcn_make_buffer_rsrc(P.ws + WS_ACT, 0, ACT_BYTES, 0x00020000);
  const int lrow0 = hf * 64;
  const int rt32 = p * 4 + hf * 2;
  if (qkv) {
    const f32x4 csq = *(const LAS f32x4*)(smem + ST_CS + (c * 16 + 4 * g) * 4), csk = *(const LAS f32x4*)(smem + ST_CS + (64 + c * 16 + 4 * g) * 4);
    const float csv = *(const LAS float*)(smem + ST_CS + (128 + c * 16 + m) * 4);
    u32x2 qf[5], kf[5], vf[5];
#pragma unroll
    for (int r = 0; r < 5; r++) {
      const float mu = *(const LAS float*)(smem + ST_MEAN + (lrow0 + 16 * r + m) * 4), rs = *(const LAS float*)(smem + ST_RSTD + (lrow0 + 16 * r + m) * 4);
      const f32x4 mu4 = *(const LAS f32x4*)(smem + ST_MEAN + (lrow0 + 16 * r + 4 * g) * 4), rs4 = *(const LAS f32x4*)(smem + ST_RSTD + (lrow0 + 16 * r + 4 * g) * 4);
      f32x4 k4 = (acc[r][1] - mu * csk) * rs, v4 = (acc[r][2] - mu4 * csv) * rs4;
      kf[r] = (u32x2){pk_bf16(k4[0], k4[1]), pk_bf16(k4[2], k4[3])};
      vf[r] = (u32x2){pk_bf16(v4[0], v4[1]), pk_bf16(v4[2], v4[3])};
      if (r > 0) { f32x4 q4 = (acc[r][0] - mu * csq) * rs; qf[r] = (u32x2){pk_bf16(q4[0], q4[1]), pk_bf16(q4[2], q4[3])}; }
    }
    const bool odd = m & 1;
    const int k0 = 4 * g + (m & 1), k1 = k0 + 2;
    const bool vc0 = k0 <= m, vc1 = k1 <= m, vp0 = k0 >= m + 2, vp1 = k1 >= m + 2;
    const unsigned pm = odd ? 0xffff0000u : 0x0000ffffu;
    const float SC = 0.25f * 1.4426950408889634f, NEG = -3.0e38f;
    const u32x4 one4 = {0x3f803f80u, 0x3f803f80u, 0x3f803f80u, 0x3f803f80u};
    u32x2 of[5];
#pragma unroll
    for (int r = 1; r < 5; r++) {
      const f32x4 z = {0.f, 0.f, 0.f, 0.f};
      f32x4 sp = __builtin_amdgcn_mfma_f32_16x16x16bf16_1k(__builtin_bit_cast(s16x4, kf[r - 1]), __builtin_bit_cast(s16x4, qf[r]), z, 0, 0, 0);
      f32x4 sc = __builtin_amdgcn_mfma_f32_16x16x16bf16_1k(__builtin_bit_cast(s16x4, kf[r]), __builtin_bit_cast(s16x4, qf[r]), z, 0, 0, 0);
      const bool prev_ok = !(r == 1 && hf == 0 && p == 0);
      float p0 = odd ? sp[1] : sp[0], p1 = odd ? sp[3] : sp[2], c0 = odd ? sc[1] : sc[0], c1 = odd ? sc[3] : sc[2];
      p0 = (vp0 && prev_ok) ? p0 : NEG; p1 = (vp1 && prev_ok) ? p1 : NEG; c0 = vc0 ? c0 : NEG; c1 = vc1 ? c1 : NEG;
      const float mx = xmax16_32(fmaxf(fmaxf(p0, p1), fmaxf(c0, c1))) * SC;
      p0 = __builtin_amdgcn_exp2f(p0 * SC - mx); p1 = __builtin_amdgcn_exp2f(p1 * SC - mx);
      c0 = __builtin_amdgcn_exp2f(c0 * SC - mx); c1 = __builtin_amdgcn_exp2f(c1 * SC - mx);
      const u32x4 pf = {pk_bf16(p0, p0) & pm, pk_bf16(p1, p1) & pm, pk_bf16(c0, c0) & pm, pk_bf16(c1, c1) & pm};
      const u32x4 vv = {vf[r - 1].x, vf[r - 1].y, vf[r].x, vf[r].y};
      f32x4 o = __builtin_amdgcn_mfma_f32_16x16x32_bf16(b8(vv), b8(pf), z, 0, 0, 0);
      f32x4 l = __builtin_amdgcn_mfma_f32_16x16x32_bf16(b8(one4), b8(pf), z, 0, 0, 0);
      const float inv = __builtin_amdgcn_rcpf(l[0]);
      of[r] = (u32x2){pk_bf16(o[0] * inv, o[1] * inv), pk_bf16(o[2] * inv, o[3] * inv)};
    }
#pragma unroll
    for (int pr = 0; pr < 2; pr++) {
      auto s0 = __builtin_amdgcn_permlane16_swap(of[1 + 2 * pr].x, of[2 + 2 * pr].x, false, false);
      auto s1 = __builtin_amdgcn_permlane16_swap(of[1 + 2 * pr].y, of[2 + 2 * pr].y, false, false);
      __builtin_amdgcn_raw_buffer_store_b128((u32x4){s0[0], s1[0], s0[1], s1[1]}, act, lane * 16 + ((rt32 + pr) * NK16 + 4 * j + c) * 1024, 0, 16);
    }
  } else {
    const int nblk = (j == 7) ? 9 : 11;
    float mu[4], rs[4];
#pragma unroll
    for (int r = 0; r < 4; r++) { mu[r] = *(const LAS float*)(smem + ST_MEAN + (lrow0 + 16 + 16 * r + m) * 4); rs[r] = *(const LAS float*)(smem + ST_RSTD + (lrow0 + 16 + 16 * r + m) * 4); }
#pragma unroll
    for (int s = 0; s < 3; s++) {
      if (4 * s + c < nblk) {
        const f32x4 cs = *(const LAS f32x4*)(smem + ST_CS + (s * 64 + c * 16 + 4 * g) * 4);
        const u32x2 b1v = *(const LAS u32x2*)(smem + KVX + ((4 * s + c) * 16 + 4 * g) * 2);
        const f32x4 bb = {bf_lo(b1v.x), bf_hi(b1v.x), bf_lo(b1v.y), bf_hi(b1v.y)};
        u32x2 hfr[4];
#pragma unroll
        for (int r = 0; r < 4; r++) {
          f32x4 v = (acc[r + 1][s] - mu[r] * cs) * rs[r] + bb;
          f32x4 zz = v * (v * v * (-0.10294324f) + (-2.3022082f));
          f32x4 h;
#pragma unroll
          for (int e = 0; e < 4; e++) h[e] = v[e] * __builtin_amdgcn_rcpf(1.f + __builtin_amdgcn_exp2f(zz[e]));
          hfr[r] = (u32x2){pk_bf16(h[0], h[1]), pk_bf16(h[2], h[3])};
        }
#pragma unroll
        for (int pr = 0; pr < 2; pr++) {
          auto s0 = __builtin_amdgcn_permlane16_swap(hfr[2 * pr].x, hfr[2 * pr + 1].x, false, false);
          auto s1 = __builtin_amdgcn_permlane16_swap(hfr[2 * pr].y, hfr[2 * pr + 1].y, false, false);
          __builtin_amdgcn_raw_buffer_store_b128((u32x4){s0[0], s1[0], s0[1], s1[1]}, act, lane * 16 + ((rt32 + pr) * NK16 + 32 + 11 * j + 4 * s + c) * 1024, 0, 16);
        }
      }
    }
  }
  STAMP(4);
  asm volatile("s_waitcnt vmcnt(0) lgkmcnt(0)" : "+s"(e_old) :: "memory");
  bar();
  const u64 tag = mix64(e_old);
  if (tid == 0) __hip_atomic_store((GAS u64*)(P.ws + WS_FLAG + p * 128 + (qkv ? j : 8 + j) * 8), tag, __ATOMIC_RELAXED, __HIP_MEMORY_SCOPE_AGENT);
  STAMP(5);
  const int q4 = m >> 2, pp = lane & 3;
  const int tb0 = c * 2048 + (8 * (g >> 1) + q4) * 128 + (((2 * (g & 1) + (pp >> 1)) ^ ((q4 >> 1) << 2)) * 16) + (pp & 1) * 8;
  u32x4 BR[10][2];
#pragma unroll
  for (int s = 0; s < 10; s++)
#pragma unroll
    for (int cb = 0; cb < 2; cb++) {
      u32x2 lo = trd(smem + b2grp(s) * 8192 + (tb0 ^ (cb * 64))), hi = trd(smem + b2grp(s) * 8192 + (tb0 ^ (cb * 64)) + 512);
      BR[s][cb] = (u32x4){lo.x, lo.y, hi.x, hi.y};
    }
  wait_lgkm0();
  bar();
#pragma unroll
  for (int s = 18; s < 30; s++) b2_issue(b2c, s);
  const int orow = rt * 64 + wave * 8 + (lane >> 3), ocol = ct * 64 + (lane & 7) * 8;
  const u32x4 xres = *(const GAS u32x4*)(P.x + orow * 1024 + ocol * 2);
  const u32x4 b2v = *(const GAS u32x4*)(P.b2 + ocol * 2);
  if (wave == 0) {
    const GAS u64* fl = (const GAS u64*)(P.ws + WS_FLAG + (rt >> 1) * 128 + (lane & 15) * 8);
    for (unsigned spin = 0; spin < SPIN_CAP; spin++) {
      u64 v = __hip_atomic_load(fl, __ATOMIC_RELAXED, __HIP_MEMORY_SCOPE_AGENT);
      if (__all(v == tag)) break;
      __builtin_amdgcn_s_sleep(1);
    }
  }
  bar();
  STAMP(6);
  f32x16 o2[2];
#pragma unroll
  for (int e = 0; e < 16; e++) { o2[0][e] = 0.f; o2[1][e] = 0.f; }
  constexpr int PF = 8;
  u32x4 AF[PF];
  const int abase = (rt * 2 + hf) * NK16;
  auto aload = [&](int s) -> u32x4 {
    const int vo = (s == 29 && b2c.nq == 29) ? 0x7f000000 : lane * 16;
    return __builtin_amdgcn_raw_buffer_load_b128(act, vo, (abase + phys_k16(b2c, s)) * 1024, 16);
  };
#pragma unroll
  for (int s = 0; s < PF; s++) AF[s] = aload(s);
#pragma unroll
  for (int s = 0; s < 30; s++) {
    if (s == 18) { wait_lgkm0(); bar(); }
    u32x4 B0, B1;
    if (s < 10) { B0 = BR[s][0]; B1 = BR[s][1]; }
    else {
      u32x2 lo = trd(smem + b2grp(s) * 8192 + tb0), hi = trd(smem + b2grp(s) * 8192 + tb0 + 512);
      B0 = (u32x4){lo.x, lo.y, hi.x, hi.y};
      lo = trd(smem + b2grp(s) * 8192 + (tb0 ^ 64)); hi = trd(smem + b2grp(s) * 8192 + (tb0 ^ 64) + 512);
      B1 = (u32x4){lo.x, lo.y, hi.x, hi.y};
    }
    const u32x4 a = AF[s % PF];
    o2[0] = __builtin_amdgcn_mfma_f32_32x32x16_bf16(b8(B0), b8(a), o2[0], 0, 0, 0);
    o2[1] = __builtin_amdgcn_mfma_f32_32x32x16_bf16(b8(B1), b8(a), o2[1], 0, 0, 0);
    if (s + PF < 30) AF[s % PF] = aload(s + PF);
    __builtin_amdgcn_sched_barrier(0);
  }
  STAMP(7);
  wait_lgkm0();
  bar();
  {
    LAS char* wp = smem + c * 17408 + (hf * 32 + (lane & 31)) * 272 + (lane >> 5) * 16;
#pragma unroll
    for (int cb = 0; cb < 2; cb++)
#pragma unroll
      for (int rq = 0; rq < 4; rq++)
        *(LAS f32x4*)(wp + cb * 128 + rq * 32) = (f32x4){o2[cb][4 * rq], o2[cb][4 * rq + 1], o2[cb][4 * rq + 2], o2[cb][4 * rq + 3]};
  }
  wait_lgkm0();
  bar();
  {
    const LAS char* rp = smem + (wave * 8 + (lane >> 3)) * 272 + (lane & 7) * 32;
    f32x4 u0 = {bf_lo(xres.x) + bf_lo(b2v.x), bf_hi(xres.x) + bf_hi(b2v.x), bf_lo(xres.y) + bf_lo(b2v.y), bf_hi(xres.y) + bf_hi(b2v.y)};
    f32x4 u1 = {bf_lo(xres.z) + bf_lo(b2v.z), bf_hi(xres.z) + bf_hi(b2v.z), bf_lo(xres.w) + bf_lo(b2v.w), bf_hi(xres.w) + bf_hi(b2v.w)};
#pragma unroll
    for (int q = 0; q < 4; q++) { u0 += *(const LAS f32x4*)(rp + q * 17408); u1 += *(const LAS f32x4*)(rp + q * 17408 + 16); }
    *(GAS u32x4*)(P.out + orow * 1024 + ocol * 2) = (u32x4){pk_bf16(u0[0], u0[1]), pk_bf16(u0[2], u0[3]), pk_bf16(u1[0], u1[1]), pk_bf16(u1[2], u1[3])};
  }
  if (b == 0 && wave == 0) {
    const GAS u64* fl = (const GAS u64*)(P.ws + WS_FLAG) + lane;
    for (unsigned spin = 0; spin < SPIN_CAP; spin++) {
      u64 v0 = __hip_atomic_load(fl, __ATOMIC_RELAXED, __HIP_MEMORY_SCOPE_AGENT), v1 = __hip_atomic_load(fl + 64, __ATOMIC_RELAXED, __HIP_MEMORY_SCOPE_AGENT);
      u64 v2 = __hip_atomic_load(fl + 128, __ATOMIC_RELAXED, __HIP_MEMORY_SCOPE_AGENT), v3 = __hip_atomic_load(fl + 192, __ATOMIC_RELAXED, __HIP_MEMORY_SCOPE_AGENT);
      if (__all(v0 == tag && v1 == tag && v2 == tag && v3 == tag)) break;
      __builtin_amdgcn_s_sleep(1);
    }
    if (lane == 0) __hip_atomic_store((GAS u64*)P.ws, tag, __ATOMIC_RELAXED, __HIP_MEMORY_SCOPE_AGENT);
  }
  STAMP(8);
  if (DIAG) { if (tid == 0) for (int i = 0; i < NSTAMP; i++) ((GAS u64*)(P.ws + WS_DIAG))[blockIdx.x * 16 + i] = stamps[i]; }
}

extern "C" void kernel_launch(void* const* d_in, const int* in_sizes, int n_in, void* d_out, int out_size, void* d_ws, size_t ws_size,
                              hipStream_t stream) {
  Params P;
  P.x = (const char*)d_in[0]; P.wqkv = (const char*)d_in[1]; P.wo = (const char*)d_in[2];
  P.w1 = (const char*)d_in[4]; P.b1 = (const char*)d_in[5]; P.w2 = (const char*)d_in[6]; P.b2 = (const char*)d_in[7];
  P.out = (char*)d_out; P.ws = (char*)d_ws;
  fused_block<<<256, 512, 0, stream>>>(P);
}
```

```cpp
#define DIAG 1
#define POSTAMP 0
#include <hip/hip_runtime.h>
#include <stdint.h>

typedef __attribute__((ext_vector_type(8))) __bf16 bf16x8;
typedef __attribute__((ext_vector_type(2))) __bf16 bf16x2;
typedef __attribute__((ext_vector_type(4))) short s16x4;
typedef __attribute__((ext_vector_type(4))) float f32x4;
typedef __attribute__((ext_vector_type(2))) float f32x2;
typedef __attribute__((ext_vector_type(16))) float f32x16;
typedef __attribute__((ext_vector_type(4))) unsigned u32x4;
typedef __attribute__((ext_vector_type(2))) unsigned u32x2;
#define LAS __attribute__((address_space(3)))
#define GAS __attribute__((address_space(1)))
typedef unsigned long long u64;

#define ABL_NS 0
#define ROT1_MODE 1
#define ROT2_MODE 1

constexpr int NK16 = 118;
constexpr int SLOT = 43008, XB = 18432;
constexpr int ST_MEAN = 129024, ST_RSTD = ST_MEAN + 576, ST_CS = ST_MEAN + 1152;
constexpr int KVX = 19 * 8192;
constexpr int WS_FLAG = 1024, WS_DIAG = 32768, WS_ACT = 65536;
constexpr int ACT_BYTES = 64 * NK16 * 1024;
constexpr int NSTAMP = 9;
constexpr unsigned SPIN_CAP = 1u << 20;

struct Params {
  const char* x; const char* wqkv; const char* wo; const char* w1; const char* b1; const char* w2; const char* b2;
  char* out; char* ws;
};

__device__ __forceinline__ void glds16(unsigned voff, const char* sbase, unsigned lds) {
  asm volatile("s_mov_b32 m0, %2\n\ts_nop 0\n\tglobal_load_lds_dwordx4 %0, %1" :: "v"(voff), "s"(sbase), "s"(lds) : "memory");
}
__device__ __forceinline__ void glds16_lo32(unsigned voff, const char* sbase, unsigned lds) {
  asm volatile("s_mov_b32 m0, %2\n\ts_mov_b32 exec_hi, 0\n\tglobal_load_lds_dwordx4 %0, %1\n\ts_mov_b32 exec_hi, -1" :: "v"(voff), "s"(sbase), "s"(lds) : "memory");
}
template <int N> __device__ __forceinline__ void wait_vm() { asm volatile("s_waitcnt vmcnt(%0) lgkmcnt(0)" :: "i"(N) : "memory"); }
__device__ __forceinline__ void wait_lgkm0() { asm volatile("s_waitcnt lgkmcnt(0)" ::: "memory"); }
__device__ __forceinline__ void bar() { asm volatile("s_barrier" ::: "memory"); }
__device__ __forceinline__ unsigned pk_bf16(float a, float b) { f32x2 p = {a, b}; return __builtin_bit_cast(unsigned, __builtin_convertvector(p, bf16x2)); }
__device__ __forceinline__ float dot2(unsigned a, unsigned b, float c) { return __builtin_amdgcn_fdot2_f32_bf16(__builtin_bit_cast(bf16x2, a), __builtin_bit_cast(bf16x2, b), c, false); }
__device__ __forceinline__ u32x2 trd(const LAS char* p) { return __builtin_bit_cast(u32x2, __builtin_amdgcn_ds_read_tr16_b64_v4i16((LAS s16x4*)p)); }
__device__ __forceinline__ bf16x8 b8(u32x4 v) { return __builtin_bit_cast(bf16x8, v); }
__device__ __forceinline__ float bf_lo(unsigned v) { return __uint_as_float(v << 16); }
__device__ __forceinline__ float bf_hi(unsigned v) { return __uint_as_float(v & 0xffff0000u); }
__device__ __forceinline__ float xmax16_32(float v) {
  auto a = __builtin_amdgcn_permlane16_swap(__float_as_uint(v), __float_as_uint(v), false, false);
  v = fmaxf(__uint_as_float(a[0]), __uint_as_float(a[1]));
  auto b = __builtin_amdgcn_permlane32_swap(__float_as_uint(v), __float_as_uint(v), false, false);
  return fmaxf(__uint_as_float(b[0]), __uint_as_float(b[1]));
}
__device__ __forceinline__ u64 mix64(u64 z) {
  z += 0x9E3779B97F4A7C15ull; z = (z ^ (z >> 30)) * 0xBF58476D1CE4E5B9ull; z = (z ^ (z >> 27)) * 0x94D049BB133111EBull; return z ^ (z >> 31);
}
#define STAMP(i) do { if (DIAG) { asm volatile("s_memrealtime %0\n\ts_waitcnt lgkmcnt(0)" : "=s"(stamps[i]) :: "memory"); } } while (0)

__device__ __forceinline__ constexpr int b2grp(int s) {
  return s < 3 ? 16 + s : s < 7 ? 8 + s : s < 18 ? s - 7 : s < 21 ? s - 2 : s < 25 ? s - 10 : s < 28 ? s - 25 : s == 28 ? 15 : 19;
}

struct B2Ctx { const char* wo; const char* w2; int kq0, nq, rot, hf, q; unsigned voff, ldsb; };
__device__ __forceinline__ int phys_k16(const B2Ctx& c, int s) { int t = s + c.rot; t = t >= c.nq ? t - c.nq : t; t = (s >= c.nq) ? 0 : t; return c.kq0 + t; }
__device__ __forceinline__ void b2_issue(const B2Ctx& c, int s) {
  int row0 = phys_k16(c, s) * 16 + c.hf * 8;
  const char* src = row0 < 512 ? c.wo + row0 * 1024 : c.w2 + (row0 - 512) * 1024;
  glds16(c.voff, src, c.ldsb + b2grp(s) * 8192 + c.q * 2048 + c.hf * 1024);
}

template <bool H0, bool QKV>
__device__ __forceinline__ void phase1(const Params& P, LAS char* smem, unsigned ldsb, int p, int j, int c, int lane, int rot, const B2Ctx& b2c,
                                       f32x4 (&acc)[5][3], u64 (&stamps)[NSTAMP]) {
  constexpr int NRB = H0 ? 5 : 4;
  constexpr int NL = H0 ? (QKV ? 5 : 4) : 6;
  const int g = lane >> 4, m = lane & 15;
  unsigned vo[6];
  const char* base0; const char* baseh = nullptr; int kstride;
  if (H0) {
#pragma unroll
    for (int i = 0; i < 4; i++) vo[i] = (32 * c + 8 * i + (lane >> 3)) * 1024 + (((lane & 7) ^ (4 * (i & 1) + (lane >> 4))) * 16);
    { int row = 4 * c + ((lane >> 3) & 3); vo[4] = row * 1024 + (((lane & 7) ^ (row >> 1)) * 16); }
    vo[5] = 0;
    base0 = P.x + p * 131072; baseh = P.x + (p > 0 ? p * 131072 - 16384 : 0); kstride = 128;
  } else {
    const int nval = (j == 7) ? 18 : 22;
#pragma unroll
    for (int i = 0; i < 6; i++) {
      int u = (c * 6 + i) * 64 + lane, row = u / 24, ch = u - row * 24, sec = ch >> 3;
      int cc = (ch & 7) ^ (2 * (((row >> 1) & 1) | ((row >> 2) & 2)));
      if (QKV) vo[i] = row * 3072 + sec * 1024 + cc * 16;
      else { int c16 = sec * 8 + cc; c16 = c16 < nval ? c16 : nval - 1; vo[i] = row * 2752 + c16 * 16; }
    }
    base0 = QKV ? P.wqkv + j * 128 : P.w1 + j * 352; kstride = QKV ? 64 * 3072 : 64 * 2752;
  }
  auto issue = [&](int ch, int slot) {
    int kc = (ch + rot) & 7;
    const char* sb = base0 + kc * kstride;
    unsigned lb = ldsb + slot * SLOT;
    if (H0) {
#pragma unroll
      for (int i = 0; i < 4; i++) glds16(vo[i], sb, lb + (16 + 32 * c + 8 * i) * 128);
      if (QKV) glds16_lo32(vo[4], baseh + kc * 128, lb + 4 * c * 128);
    } else {
#pragma unroll
      for (int i = 0; i < 6; i++) glds16(vo[i], sb, lb + XB + (c * 6 + i) * 1024);
    }
  };
  issue(0, 0); issue(1, 1); issue(2, 2);
  if (H0 && !QKV) { if (c == 0) { int l = lane < (j == 7 ? 18 : 22) ? lane : 0; glds16(l * 16, P.b1 + j * 352, ldsb + KVX); } }
  const int a0 = (H0 ? 0 : 5 * 2048) + m * 128 + ((g ^ (m >> 1)) * 16);
  const int q4 = m >> 2, pp = lane & 3;
  const int b0 = XB + (8 * g + q4) * 384 + (((2 * c + (pp >> 1)) ^ (2 * ((q4 >> 1) | ((g & 1) << 1)))) * 16) + (pp & 1) * 8;
  const int w = (H0 ? 0 : 4) + c;
  const int s0 = (16 + 16 * w) * 128 + lane * 16;
  const int sh = 2 * w * 128 + (lane & 15) * 16;
  const bool ones = QKV ? (m < 2) : true;
  float sm[3] = {0.f, 0.f, 0.f}, sq[3] = {0.f, 0.f, 0.f};
#pragma unroll
  for (int r = 0; r < 5; r++)
#pragma unroll
    for (int s = 0; s < 3; s++) acc[r][s] = (f32x4){0.f, 0.f, 0.f, 0.f};
  int slot = 0;
  for (int ch = 0; ch < 8; ch++) {
    if (ch == 0) wait_vm<2 * NL>(); else if (ch < 6) wait_vm<NL>(); else if (ch == 6) wait_vm<NL + 3>(); else wait_vm<7>();
    bar();
    if (ch == 0) STAMP(1);
    if (ch >= 1 && ch <= 5) issue(ch + 2, slot == 0 ? 2 : slot - 1);
    if (ch == 5) { b2_issue(b2c, 0); b2_issue(b2c, 1); b2_issue(b2c, 2); }
    if (ch == 6) { b2_issue(b2c, 3); b2_issue(b2c, 4); b2_issue(b2c, 5); b2_issue(b2c, 6); }
    if (ch == 7) { b2_issue(b2c, 7); b2_issue(b2c, 8); b2_issue(b2c, 9); b2_issue(b2c, 10); b2_issue(b2c, 11); }
    LAS char* sb = smem + slot * SLOT;
#pragma unroll
    for (int ks = 0; ks < 2; ks++) {
      u32x4 B[3], A[5];
#pragma unroll
      for (int s = 0; s < 3; s++) {
        u32x2 lo = trd(sb + b0 + (32 * ks) * 384 + s * 128), hi = trd(sb + b0 + (32 * ks + 4) * 384 + s * 128);
        B[s] = (u32x4){lo.x, lo.y, hi.x, hi.y};
      }
#pragma unroll
      for (int r = 0; r < NRB; r++) {
        if (H0 && !QKV && r == 0) A[r] = (u32x4){0x3f803f80u, 0x3f803f80u, 0x3f803f80u, 0x3f803f80u};
        else A[r] = *(const LAS u32x4*)(sb + (a0 ^ (ks * 64)) + r * 2048);
      }
      if (H0 && QKV) {
#pragma unroll
        for (int e = 0; e < 4; e++) A[0][e] = ones ? 0x3f803f80u : A[0][e];
      }
      u32x4 T = *(const LAS u32x4*)(sb + s0 + ks * 1024);
#pragma unroll
      for (int e = 0; e < 4; e++) { sm[ks] = dot2(T[e], 0x3f803f80u, sm[ks]); sq[ks] = dot2(T[e], T[e], sq[ks]); }
      if (QKV && ks == 0 && !ABL_NS) {
        u32x4 Hh = *(const LAS u32x4*)(sb + sh);
#pragma unroll
        for (int e = 0; e < 4; e++) { sm[2] = dot2(Hh[e], 0x3f803f80u, sm[2]); sq[2] = dot2(Hh[e], Hh[e], sq[2]); }
      }
#pragma unroll
      for (int r = 0; r < NRB; r++)
#pragma unroll
        for (int s = 0; s < 3; s++) {
          if (QKV && s == 2) acc[r][s] = __builtin_amdgcn_mfma_f32_16x16x32_bf16(b8(A[r]), b8(B[s]), acc[r][s], 0, 0, 0);
          else acc[r][s] = __builtin_amdgcn_mfma_f32_16x16x32_bf16(b8(B[s]), b8(A[r]), acc[r][s], 0, 0, 0);
        }
    }
    slot = slot == 2 ? 0 : slot + 1;
  }
  STAMP(2);
#pragma unroll
  for (int i = 0; i < (QKV ? 3 : 2); i++) {
    float a = sm[i], b = sq[i];
#pragma unroll
    for (int d = 1; d < 8; d <<= 1) { a += __shfl_xor(a, d); b += __shfl_xor(b, d); }
    float mean = a * (1.f / 512.f), var = fmaxf(b * (1.f / 512.f) - mean * mean, 0.f), rs = __builtin_amdgcn_rsqf(var + 1e-5f);
    int row = i < 2 ? 16 + 16 * w + 8 * i + (lane >> 3) : 2 * w + ((lane >> 3) & 1);
    if ((lane & 7) == 0) { *(LAS float*)(smem + ST_MEAN + row * 4) = mean; *(LAS float*)(smem + ST_RSTD + row * 4) = rs; }
  }
  if (H0) {
    if (m == 0) {
      *(LAS f32x4*)(smem + ST_CS + (0 * 64 + c * 16 + 4 * g) * 4) = acc[0][0];
      *(LAS f32x4*)(smem + ST_CS + (1 * 64 + c * 16 + 4 * g) * 4) = acc[0][1];
      if (!QKV) *(LAS f32x4*)(smem + ST_CS + (2 * 64 + c * 16 + 4 * g) * 4) = acc[0][2];
    }
    if (QKV) {
      if (g == 0) *(LAS float*)(smem + ST_CS + (2 * 64 + c * 16 + m) * 4) = acc[0][2][0];
      *(LAS f32x4*)(smem + KVX + c * 2048 + lane * 16) = acc[4][1];
      *(LAS f32x4*)(smem + KVX + c * 2048 + 1024 + lane * 16) = acc[4][2];
    }
  } else {
#pragma unroll
    for (int r = 4; r >= 1; r--)
#pragma unroll
      for (int s = 0; s < 3; s++) acc[r][s] = acc[r - 1][s];
  }
  wait_lgkm0();
  bar();
  if (!H0 && QKV) {
    acc[0][1] = *(const LAS f32x4*)(smem + KVX + c * 2048 + lane * 16);
    acc[0][2] = *(const LAS f32x4*)(smem + KVX + c * 2048 + 1024 + lane * 16);
  }
}

__global__ __launch_bounds__(512, 2) void fused_block(Params P) {
  __shared__ __attribute__((aligned(16))) char smem_[163840];
  LAS char* smem = (LAS char*)smem_;
  const unsigned ldsb = (unsigned)(size_t)smem;
  u64 stamps[NSTAMP];
  STAMP(0);
  const int tid = threadIdx.x, lane = tid & 63, wave = __builtin_amdgcn_readfirstlane(tid >> 6);
  const int hf = wave >> 2, c = wave & 3, g = lane >> 4, m = lane & 15;
  const int b = blockIdx.x, xcd = b & 7, r5 = b >> 3;
  const int p = (xcd & 1) * 8 + (r5 & 7), jj = r5 >> 3, j = (xcd >> 1) * 2 + (jj & 1);
  const bool qkv = jj < 2;
  const int rot1 = ROT1_MODE == 0 ? 0 : ROT1_MODE == 1 ? (p + jj) : ROT1_MODE == 2 ? ((p + jj) & 1) * 4 : ROT1_MODE == 4 ? ((p + jj) & 3) * 2 : (p + 2 * jj);
  const int rt = xcd * 4 + jj, ct = r5 & 7;

  B2Ctx b2c;
  b2c.wo = P.wo + ct * 128; b2c.w2 = P.w2 + ct * 128; b2c.hf = hf; b2c.q = c; b2c.ldsb = ldsb;
  b2c.kq0 = c < 3 ? 30 * c : 89; b2c.nq = c < 2 ? 30 : 29;
  b2c.rot = ROT2_MODE == 0 ? 0 : ROT2_MODE == 1 ? ((ct + rt) & 1) * 15 : ROT2_MODE == 3 ? ((ct + rt) % 3) * 10 : ((ct + rt) & 3) * 7;
  b2c.voff = (lane >> 3) * 1024 + (((lane & 7) ^ (((lane >> 4) & 1) << 2)) * 16);

  f32x4 acc[5][3];
  if (qkv) { if (hf == 0) phase1<true, true>(P, smem, ldsb, p, j, c, lane, rot1, b2c, acc, stamps); else phase1<false, true>(P, smem, ldsb, p, j, c, lane, rot1, b2c, acc, stamps); }
  else     { if (hf == 0) phase1<true, false>(P, smem, ldsb, p, j, c, lane, rot1, b2c, acc, stamps); else phase1<false, false>(P, smem, ldsb, p, j, c, lane, rot1, b2c, acc, stamps); }
  STAMP(3);
  u64 e_old;
  asm volatile("s_load_dwordx2 %0, %1, 0x0" : "=s"(e_old) : "s"(P.ws) : "memory");
#pragma unroll
  for (int s = 12; s < 18; s++) b2_issue(b2c, s);

  const __amdgpu_buffer_rsrc_t act = __builtin_amdgcn_make_buffer_rsrc(P.ws + WS_ACT, 0, ACT_BYTES, 0x00020000);
  const int lrow0 = hf * 64;
  const int rt32 = p * 4 + hf * 2;
  if (qkv) {
    const f32x4 csq = *(const LAS f32x4*)(smem + ST_CS + (c * 16 + 4 * g) * 4), csk = *(const LAS f32x4*)(smem + ST_CS + (64 + c * 16 + 4 * g) * 4);
    const float csv = *(const LAS float*)(smem + ST_CS + (128 + c * 16 + m) * 4);
    u32x2 qf[5], kf[5], vf[5];
#pragma unroll
    for (int r = 0; r < 5; r++) {
      const float mu = *(const LAS float*)(smem + ST_MEAN + (lrow0 + 16 * r + m) * 4), rs = *(const LAS float*)(smem + ST_RSTD + (lrow0 + 16 * r + m) * 4);
      const f32x4 mu4 = *(const LAS f32x4*)(smem + ST_MEAN + (lrow0 + 16 * r + 4 * g) * 4), rs4 = *(const LAS f32x4*)(smem + ST_RSTD + (lrow0 + 16 * r + 4 * g) * 4);
      f32x4 k4 = (acc[r][1] - mu * csk) * rs, v4 = (acc[r][2] - mu4 * csv) * rs4;
      kf[r] = (u32x2){pk_bf16(k4[0], k4[1]), pk_bf16(k4[2], k4[3])};
      vf[r] = (u32x2){pk_bf16(v4[0], v4[1]), pk_bf16(v4[2], v4[3])};
      if (r > 0) { f32x4 q4 = (acc[r][0] - mu * csq) * rs; qf[r] = (u32x2){pk_bf16(q4[0], q4[1]), pk_bf16(q4[2], q4[3])}; }
    }
    const bool odd = m & 1;
    const int k0 = 4 * g + (m & 1), k1 = k0 + 2;
    const bool vc0 = k0 <= m, vc1 = k1 <= m, vp0 = k0 >= m + 2, vp1 = k1 >= m + 2;
    const unsigned pm = odd ? 0xffff0000u : 0x0000ffffu;
    const float SC = 0.25f * 1.4426950408889634f, NEG = -3.0e38f;
    const u32x4 one4 = {0x3f803f80u, 0x3f803f80u, 0x3f803f80u, 0x3f803f80u};
    u32x2 of[5];
#pragma unroll
    for (int r = 1; r < 5; r++) {
      const f32x4 z = {0.f, 0.f, 0.f, 0.f};
      f32x4 sp = __builtin_amdgcn_mfma_f32_16x16x16bf16_1k(__builtin_bit_cast(s16x4, kf[r - 1]), __builtin_bit_cast(s16x4, qf[r]), z, 0, 0, 0);
      f32x4 sc = __builtin_amdgcn_mfma_f32_16x16x16bf16_1k(__builtin_bit_cast(s16x4, kf[r]), __builtin_bit_cast(s16x4, qf[r]), z, 0, 0, 0);
      const bool prev_ok = !(r == 1 && hf == 0 && p == 0);
      float p0 = odd ? sp[1] : sp[0], p1 = odd ? sp[3] : sp[2], c0 = odd ? sc[1] : sc[0], c1 = odd ? sc[3] : sc[2];
      p0 = (vp0 && prev_ok) ? p0 : NEG; p1 = (vp1 && prev_ok) ? p1 : NEG; c0 = vc0 ? c0 : NEG; c1 = vc1 ? c1 : NEG;
      const float mx = xmax16_32(fmaxf(fmaxf(p0, p1), fmaxf(c0, c1))) * SC;
      p0 = __builtin_amdgcn_exp2f(p0 * SC - mx); p1 = __builtin_amdgcn_exp2f(p1 * SC - mx);
      c0 = __builtin_amdgcn_exp2f(c0 * SC - mx); c1 = __builtin_amdgcn_exp2f(c1 * SC - mx);
      const u32x4 pf = {pk_bf16(p0, p0) & pm, pk_bf16(p1, p1) & pm, pk_bf16(c0, c0) & pm, pk_bf16(c1, c1) & pm};
      const u32x4 vv = {vf[r - 1].x, vf[r - 1].y, vf[r].x, vf[r].y};
      f32x4 o = __builtin_amdgcn_mfma_f32_16x16x32_bf16(b8(vv), b8(pf), z, 0, 0, 0);
      f32x4 l = __builtin_amdgcn_mfma_f32_16x16x32_bf16(b8(one4), b8(pf), z, 0, 0, 0);
      const float inv = __builtin_amdgcn_rcpf(l[0]);
      of[r] = (u32x2){pk_bf16(o[0] * inv, o[1] * inv), pk_bf16(o[2] * inv, o[3] * inv)};
    }
#pragma unroll
    for (int pr = 0; pr < 2; pr++) {
      auto s0 = __builtin_amdgcn_permlane16_swap(of[1 + 2 * pr].x, of[2 + 2 * pr].x, false, false);
      auto s1 = __builtin_amdgcn_permlane16_swap(of[1 + 2 * pr].y, of[2 + 2 * pr].y, false, false);
      __builtin_amdgcn_raw_buffer_store_b128((u32x4){s0[0], s1[0], s0[1], s1[1]}, act, lane * 16 + ((rt32 + pr) * NK16 + 4 * j + c) * 1024, 0, 16);
    }
  } else {
    const int nblk = (j == 7) ? 9 : 11;
    float mu[4], rs[4];
#pragma unroll
    for (int r = 0; r < 4; r++) { mu[r] = *(const LAS float*)(smem + ST_MEAN + (lrow0 + 16 + 16 * r + m) * 4); rs[r] = *(const LAS float*)(smem + ST_RSTD + (lrow0 + 16 + 16 * r + m) * 4); }
#pragma unroll
    for (int s = 0; s < 3; s++) {
      if (4 * s + c < nblk) {
        const f32x4 cs = *(const LAS f32x4*)(smem + ST_CS + (s * 64 + c * 16 + 4 * g) * 4);
        const u32x2 b1v = *(const LAS u32x2*)(smem + KVX + ((4 * s + c) * 16 + 4 * g) * 2);
        const f32x4 bb = {bf_lo(b1v.x), bf_hi(b1v.x), bf_lo(b1v.y), bf_hi(b1v.y)};
        u32x2 hfr[4];
#pragma unroll
        for (int r = 0; r < 4; r++) {
          f32x4 v = (acc[r + 1][s] - mu[r] * cs) * rs[r] + bb;
          f32x4 zz = v * (v * v * (-0.10294324f) + (-2.3022082f));
          f32x4 h;
#pragma unroll
          for (int e = 0; e < 4; e++) h[e] = v[e] * __builtin_amdgcn_rcpf(1.f + __builtin_amdgcn_exp2f(zz[e]));
          hfr[r] = (u32x2){pk_bf16(h[0], h[1]), pk_bf16(h[2], h[3])};
        }
#pragma unroll
        for (int pr = 0; pr < 2; pr++) {
          auto s0 = __builtin_amdgcn_permlane16_swap(hfr[2 * pr].x, hfr[2 * pr + 1].x, false, false);
          auto s1 = __builtin_amdgcn_permlane16_swap(hfr[2 * pr].y, hfr[2 * pr + 1].y, false, false);
          __builtin_amdgcn_raw_buffer_store_b128((u32x4){s0[0], s1[0], s0[1], s1[1]}, act, lane * 16 + ((rt32 + pr) * NK16 + 32 + 11 * j + 4 * s + c) * 1024, 0, 16);
        }
      }
    }
  }
  STAMP(4);
  asm volatile("s_waitcnt vmcnt(0) lgkmcnt(0)" : "+s"(e_old) :: "memory");
  bar();
  const u64 tag = mix64(e_old);
  if (tid == 0) __hip_atomic_store((GAS u64*)(P.ws + WS_FLAG + p * 128 + (qkv ? j : 8 + j) * 8), tag, __ATOMIC_RELAXED, __HIP_MEMORY_SCOPE_AGENT);
  STAMP(5);
  const int q4 = m >> 2, pp = lane & 3;
  const int tb0 = c * 2048 + (8 * (g >> 1) + q4) * 128 + (((2 * (g & 1) + (pp >> 1)) ^ ((q4 >> 1) << 2)) * 16) + (pp & 1) * 8;
  u32x4 BR[10][2];
#pragma unroll
  for (int s = 0; s < 10; s++)
#pragma unroll
    for (int cb = 0; cb < 2; cb++) {
      u32x2 lo = trd(smem + b2grp(s) * 8192 + (tb0 ^ (cb * 64))), hi = trd(smem + b2grp(s) * 8192 + (tb0 ^ (cb * 64)) + 512);
      BR[s][cb] = (u32x4){lo.x, lo.y, hi.x, hi.y};
    }
  wait_lgkm0();
  bar();
#pragma unroll
  for (int s = 18; s < 30; s++) b2_issue(b2c, s);
  const int orow = rt * 64 + wave * 8 + (lane >> 3), ocol = ct * 64 + (lane & 7) * 8;
  const u32x4 xres = *(const GAS u32x4*)(P.x + orow * 1024 + ocol * 2);
  const u32x4 b2v = *(const GAS u32x4*)(P.b2 + ocol * 2);
  if (wave == 0) {
    const GAS u64* fl = (const GAS u64*)(P.ws + WS_FLAG + (rt >> 1) * 128 + (lane & 15) * 8);
    for (unsigned spin = 0; spin < SPIN_CAP; spin++) {
      u64 v = __hip_atomic_load(fl, __ATOMIC_RELAXED, __HIP_MEMORY_SCOPE_AGENT);
      if (__all(v == tag)) break;
      __builtin_amdgcn_s_sleep(1);
    }
  }
  bar();
  STAMP(6);
  f32x16 o2[2];
#pragma unroll
  for (int e = 0; e < 16; e++) { o2[0][e] = 0.f; o2[1][e] = 0.f; }
  constexpr int PF = 8;
  u32x4 AF[PF];
  const int abase = (rt * 2 + hf) * NK16;
  auto aload = [&](int s) -> u32x4 {
    const int vo = (s == 29 && b2c.nq == 29) ? 0x7f000000 : lane * 16;
    return __builtin_amdgcn_raw_buffer_load_b128(act, vo, (abase + phys_k16(b2c, s)) * 1024, 16);
  };
#pragma unroll
  for (int s = 0; s < PF; s++) AF[s] = aload(s);
#pragma unroll
  for (int s = 0; s < 30; s++) {
    if (s == 18) { wait_lgkm0(); bar(); }
    u32x4 B0, B1;
    if (s < 10) { B0 = BR[s][0]; B1 = BR[s][1]; }
    else {
      u32x2 lo = trd(smem + b2grp(s) * 8192 + tb0), hi = trd(smem + b2grp(s) * 8192 + tb0 + 512);
      B0 = (u32x4){lo.x, lo.y, hi.x, hi.y};
      lo = trd(smem + b2grp(s) * 8192 + (tb0 ^ 64)); hi = trd(smem + b2grp(s) * 8192 + (tb0 ^ 64) + 512);
      B1 = (u32x4){lo.x, lo.y, hi.x, hi.y};
    }
    const u32x4 a = AF[s % PF];
    o2[0] = __builtin_amdgcn_mfma_f32_32x32x16_bf16(b8(B0), b8(a), o2[0], 0, 0, 0);
    o2[1] = __builtin_amdgcn_mfma_f32_32x32x16_bf16(b8(B1), b8(a), o2[1], 0, 0, 0);
    if (s + PF < 30) AF[s % PF] = aload(s + PF);
    __builtin_amdgcn_sched_barrier(0);
  }
  STAMP(7);
  wait_lgkm0();
  bar();
  {
    LAS char* wp = smem + c * 17408 + (hf * 32 + (lane & 31)) * 272 + (lane >> 5) * 16;
#pragma unroll
    for (int cb = 0; cb < 2; cb++)
#pragma unroll
      for (int rq = 0; rq < 4; rq++)
        *(LAS f32x4*)(wp + cb * 128 + rq * 32) = (f32x4){o2[cb][4 * rq], o2[cb][4 * rq + 1], o2[cb][4 * rq + 2], o2[cb][4 * rq + 3]};
  }
  wait_lgkm0();
  bar();
  {
    const LAS char* rp = smem + (wave * 8 + (lane >> 3)) * 272 + (lane & 7) * 32;
    f32x4 u0 = {bf_lo(xres.x) + bf_lo(b2v.x), bf_hi(xres.x) + bf_hi(b2v.x), bf_lo(xres.y) + bf_lo(b2v.y), bf_hi(xres.y) + bf_hi(b2v.y)};
    f32x4 u1 = {bf_lo(xres.z) + bf_lo(b2v.z), bf_hi(xres.z) + bf_hi(b2v.z), bf_lo(xres.w) + bf_lo(b2v.w), bf_hi(xres.w) + bf_hi(b2v.w)};
#pragma unroll
    for (int q = 0; q < 4; q++) { u0 += *(const LAS f32x4*)(rp + q * 17408); u1 += *(const LAS f32x4*)(rp + q * 17408 + 16); }
    *(GAS u32x4*)(P.out + orow * 1024 + ocol * 2) = (u32x4){pk_bf16(u0[0], u0[1]), pk_bf16(u0[2], u0[3]), pk_bf16(u1[0], u1[1]), pk_bf16(u1[2], u1[3])};
  }
  if (b == 0 && wave == 0) {
    const GAS u64* fl = (const GAS u64*)(P.ws + WS_FLAG) + lane;
    for (unsigned spin = 0; spin < SPIN_CAP; spin++) {
      u64 v0 = __hip_atomic_load(fl, __ATOMIC_RELAXED, __HIP_MEMORY_SCOPE_AGENT), v1 = __hip_atomic_load(fl + 64, __ATOMIC_RELAXED, __HIP_MEMORY_SCOPE_AGENT);
      u64 v2 = __hip_atomic_load(fl + 128, __ATOMIC_RELAXED, __HIP_MEMORY_SCOPE_AGENT), v3 = __hip_atomic_load(fl + 192, __ATOMIC_RELAXED, __HIP_MEMORY_SCOPE_AGENT);
      if (__all(v0 == tag && v1 == tag && v2 == tag && v3 == tag)) break;
      __builtin_amdgcn_s_sleep(1);
    }
    if (lane == 0) __hip_atomic_store((GAS u64*)P.ws, tag, __ATOMIC_RELAXED, __HIP_MEMORY_SCOPE_AGENT);
  }
  STAMP(8);
  if (DIAG) { if (tid == 0) for (int i = 0; i < NSTAMP; i++) ((GAS u64*)(P.ws + WS_DIAG))[blockIdx.x * 16 + i] = stamps[i]; }
}

__global__ void pre_k(char* ws) { if (threadIdx.x == 0 && blockIdx.x == 0) *(GAS u64*)(ws + 512) = __builtin_amdgcn_s_memrealtime(); }
__global__ void post_k(char* ws, int amp) {
  u64 now = __builtin_amdgcn_s_memrealtime();
  u64 v = ((const GAS u64*)(ws + WS_DIAG))[threadIdx.x * 16 + 8];
  for (int d = 1; d < 64; d <<= 1) { u64 o = __shfl_xor(v, d); v = o > v ? o : v; }
  __shared__ u64 sm[4]; if ((threadIdx.x & 63) == 0) sm[threadIdx.x >> 6] = v; __syncthreads();
  v = sm[0]; for (int i = 1; i < 4; i++) v = sm[i] > v ? sm[i] : v;
  u64 until = now + amp * (now - v); while (__builtin_amdgcn_s_memrealtime() < until) __builtin_amdgcn_s_sleep(2);
}
extern "C" void kernel_launch(void* const* d_in, const int* in_sizes, int n_in, void* d_out, int out_size, void* d_ws, size_t ws_size,
                              hipStream_t stream) {
  Params P;
  P.x = (const char*)d_in[0]; P.wqkv = (const char*)d_in[1]; P.wo = (const char*)d_in[2];
  P.w1 = (const char*)d_in[4]; P.b1 = (const char*)d_in[5]; P.w2 = (const char*)d_in[6]; P.b2 = (const char*)d_in[7];
  P.out = (char*)d_out; P.ws = (char*)d_ws;
  fused_block<<<256, 512, 0, stream>>>(P);
  post_k<<<1, 256, 0, stream>>>(P.ws, POSTAMP);
}
```

```cpp
#define DIAG 1
#define PREAMP 0
#define PREGRID 256
#define PREWB
#include <hip/hip_runtime.h>
#include <stdint.h>

typedef __attribute__((ext_vector_type(8))) __bf16 bf16x8;
typedef __attribute__((ext_vector_type(2))) __bf16 bf16x2;
typedef __attribute__((ext_vector_type(4))) short s16x4;
typedef __attribute__((ext_vector_type(4))) float f32x4;
typedef __attribute__((ext_vector_type(2))) float f32x2;
typedef __attribute__((ext_vector_type(16))) float f32x16;
typedef __attribute__((ext_vector_type(4))) unsigned u32x4;
typedef __attribute__((ext_vector_type(2))) unsigned u32x2;
#define LAS __attribute__((address_space(3)))
#define GAS __attribute__((address_space(1)))
typedef unsigned long long u64;

#define ABL_NS 0
#define ROT1_MODE 1
#define ROT2_MODE 1

constexpr int NK16 = 118;
constexpr int SLOT = 43008, XB = 18432;
constexpr int ST_MEAN = 129024, ST_RSTD = ST_MEAN + 576, ST_CS = ST_MEAN + 1152;
constexpr int KVX = 19 * 8192;
constexpr int WS_FLAG = 1024, WS_DIAG = 32768, WS_ACT = 65536;
constexpr int ACT_BYTES = 64 * NK16 * 1024;
constexpr int NSTAMP = 9;
constexpr unsigned SPIN_CAP = 1u << 20;

struct Params {
  const char* x; const char* wqkv; const char* wo; const char* w1; const char* b1; const char* w2; const char* b2;
  char* out; char* ws;
};

__device__ __forceinline__ void glds16(unsigned voff, const char* sbase, unsigned lds) {
  asm volatile("s_mov_b32 m0, %2\n\ts_nop 0\n\tglobal_load_lds_dwordx4 %0, %1" :: "v"(voff), "s"(sbase), "s"(lds) : "memory");
}
__device__ __forceinline__ void glds16_lo32(unsigned voff, const char* sbase, unsigned lds) {
  asm volatile("s_mov_b32 m0, %2\n\ts_mov_b32 exec_hi, 0\n\tglobal_load_lds_dwordx4 %0, %1\n\ts_mov_b32 exec_hi, -1" :: "v"(voff), "s"(sbase), "s"(lds) : "memory");
}
template <int N> __device__ __forceinline__ void wait_vm() { asm volatile("s_waitcnt vmcnt(%0) lgkmcnt(0)" :: "i"(N) : "memory"); }
__device__ __forceinline__ void wait_lgkm0() { asm volatile("s_waitcnt lgkmcnt(0)" ::: "memory"); }
__device__ __forceinline__ void bar() { asm volatile("s_barrier" ::: "memory"); }
__device__ __forceinline__ unsigned pk_bf16(float a, float b) { f32x2 p = {a, b}; return __builtin_bit_cast(unsigned, __builtin_convertvector(p, bf16x2)); }
__device__ __forceinline__ float dot2(unsigned a, unsigned b, float c) { return __builtin_amdgcn_fdot2_f32_bf16(__builtin_bit_cast(bf16x2, a), __builtin_bit_cast(bf16x2, b), c, false); }
__device__ __forceinline__ u32x2 trd(const LAS char* p) { return __builtin_bit_cast(u32x2, __builtin_amdgcn_ds_read_tr16_b64_v4i16((LAS s16x4*)p)); }
__device__ __forceinline__ bf16x8 b8(u32x4 v) { return __builtin_bit_cast(bf16x8, v); }
__device__ __forceinline__ float bf_lo(unsigned v) { return __uint_as_float(v << 16); }
__device__ __forceinline__ float bf_hi(unsigned v) { return __uint_as_float(v & 0xffff0000u); }
__device__ __forceinline__ float xmax16_32(float v) {
  auto a = __builtin_amdgcn_permlane16_swap(__float_as_uint(v), __float_as_uint(v), false, false);
  v = fmaxf(__uint_as_float(a[0]), __uint_as_float(a[1]));
  auto b = __builtin_amdgcn_permlane32_swap(__float_as_uint(v), __float_as_uint(v), false, false);
  return fmaxf(__uint_as_float(b[0]), __uint_as_float(b[1]));
}
__device__ __forceinline__ u64 mix64(u64 z) {
  z += 0x9E3779B97F4A7C15ull; z = (z ^ (z >> 30)) * 0xBF58476D1CE4E5B9ull; z = (z ^ (z >> 27)) * 0x94D049BB133111EBull; return z ^ (z >> 31);
}
#define STAMP(i) do { if (DIAG) { asm volatile("s_memrealtime %0\n\ts_waitcnt lgkmcnt(0)" : "=s"(stamps[i]) :: "memory"); } } while (0)

__device__ __forceinline__ constexpr int b2grp(int s) {
  return s < 3 ? 16 + s : s < 7 ? 8 + s : s < 18 ? s - 7 : s < 21 ? s - 2 : s < 25 ? s - 10 : s < 28 ? s - 25 : s == 28 ? 15 : 19;
}

struct B2Ctx { const char* wo; const char* w2; int kq0, nq, rot, hf, q; unsigned voff, ldsb; };
__device__ __forceinline__ int phys_k16(const B2Ctx& c, int s) { int t = s + c.rot; t = t >= c.nq ? t - c.nq : t; t = (s >= c.nq) ? 0 : t; return c.kq0 + t; }
__device__ __forceinline__ void b2_issue(const B2Ctx& c, int s) {
  int row0 = phys_k16(c, s) * 16 + c.hf * 8;
  const char* src = row0 < 512 ? c.wo + row0 * 1024 : c.w2 + (row0 - 512) * 1024;
  glds16(c.voff, src, c.ldsb + b2grp(s) * 8192 + c.q * 2048 + c.hf * 1024);
}

template <bool H0, bool QKV>
__device__ __forceinline__ void phase1(const Params& P, LAS char* smem, unsigned ldsb, int p, int j, int c, int lane, int rot, const B2Ctx& b2c,
                                       f32x4 (&acc)[5][3], u64 (&stamps)[NSTAMP]) {
  constexpr int NRB = H0 ? 5 : 4;
  constexpr int NL = H0 ? (QKV ? 5 : 4) : 6;
  const int g = lane >> 4, m = lane & 15;
  unsigned vo[6];
  const char* base0; const char* baseh = nullptr; int kstride;
  if (H0) {
#pragma unroll
    for (int i = 0; i < 4; i++) vo[i] = (32 * c + 8 * i + (lane >> 3)) * 1024 + (((lane & 7) ^ (4 * (i & 1) + (lane >> 4))) * 16);
    { int row = 4 * c + ((lane >> 3) & 3); vo[4] = row * 1024 + (((lane & 7) ^ (row >> 1)) * 16); }
    vo[5] = 0;
    base0 = P.x + p * 131072; baseh = P.x + (p > 0 ? p * 131072 - 16384 : 0); kstride = 128;
  } else {
    const int nval = (j == 7) ? 18 : 22;
#pragma unroll
    for (int i = 0; i < 6; i++) {
      int u = (c * 6 + i) * 64 + lane, row = u / 24, ch = u - row * 24, sec = ch >> 3;
      int cc = (ch & 7) ^ (2 * (((row >> 1) & 1) | ((row >> 2) & 2)));
      if (QKV) vo[i] = row * 3072 + sec * 1024 + cc * 16;
      else { int c16 = sec * 8 + cc; c16 = c16 < nval ? c16 : nval - 1; vo[i] = row * 2752 + c16 * 16; }
    }
    base0 = QKV ? P.wqkv + j * 128 : P.w1 + j * 352; kstride = QKV ? 64 * 3072 : 64 * 2752;
  }
  auto issue = [&](int ch, int slot) {
    int kc = (ch + rot) & 7;
    const char* sb = base0 + kc * kstride;
    unsigned lb = ldsb + slot * SLOT;
    if (H0) {
#pragma unroll
      for (int i = 0; i < 4; i++) glds16(vo[i], sb, lb + (16 + 32 * c + 8 * i) * 128);
      if (QKV) glds16_lo32(vo[4], baseh + kc * 128, lb + 4 * c * 128);
    } else {
#pragma unroll
      for (int i = 0; i < 6; i++) glds16(vo[i], sb, lb + XB + (c * 6 + i) * 1024);
    }
  };
  issue(0, 0); issue(1, 1); issue(2, 2);
  if (H0 && !QKV) { if (c == 0) { int l = lane < (j == 7 ? 18 : 22) ? lane : 0; glds16(l * 16, P.b1 + j * 352, ldsb + KVX); } }
  const int a0 = (H0 ? 0 : 5 * 2048) + m * 128 + ((g ^ (m >> 1)) * 16);
  const int q4 = m >> 2, pp = lane & 3;
  const int b0 = XB + (8 * g + q4) * 384 + (((2 * c + (pp >> 1)) ^ (2 * ((q4 >> 1) | ((g & 1) << 1)))) * 16) + (pp & 1) * 8;
  const int w = (H0 ? 0 : 4) + c;
  const int s0 = (16 + 16 * w) * 128 + lane * 16;
  const int sh = 2 * w * 128 + (lane & 15) * 16;
  const bool ones = QKV ? (m < 2) : true;
  float sm[3] = {0.f, 0.f, 0.f}, sq[3] = {0.f, 0.f, 0.f};
#pragma unroll
  for (int r = 0; r < 5; r++)
#pragma unroll
    for (int s = 0; s < 3; s++) acc[r][s] = (f32x4){0.f, 0.f, 0.f, 0.f};
  int slot = 0;
  for (int ch = 0; ch < 8; ch++) {
    if (ch == 0) wait_vm<2 * NL>(); else if (ch < 6) wait_vm<NL>(); else if (ch == 6) wait_vm<NL + 3>(); else wait_vm<7>();
    bar();
    if (ch == 0) STAMP(1);
    if (ch >= 1 && ch <= 5) issue(ch + 2, slot == 0 ? 2 : slot - 1);
    if (ch == 5) { b2_issue(b2c, 0); b2_issue(b2c, 1); b2_issue(b2c, 2); }
    if (ch == 6) { b2_issue(b2c, 3); b2_issue(b2c, 4); b2_issue(b2c, 5); b2_issue(b2c, 6); }
    if (ch == 7) { b2_issue(b2c, 7); b2_issue(b2c, 8); b2_issue(b2c, 9); b2_issue(b2c, 10); b2_issue(b2c, 11); }
    LAS char* sb = smem + slot * SLOT;
#pragma unroll
    for (int ks = 0; ks < 2; ks++) {
      u32x4 B[3], A[5];
#pragma unroll
      for (int s = 0; s < 3; s++) {
        u32x2 lo = trd(sb + b0 + (32 * ks) * 384 + s * 128), hi = trd(sb + b0 + (32 * ks + 4) * 384 + s * 128);
        B[s] = (u32x4){lo.x, lo.y, hi.x, hi.y};
      }
#pragma unroll
      for (int r = 0; r < NRB; r++) {
        if (H0 && !QKV && r == 0) A[r] = (u32x4){0x3f803f80u, 0x3f803f80u, 0x3f803f80u, 0x3f803f80u};
        else A[r] = *(const LAS u32x4*)(sb + (a0 ^ (ks * 64)) + r * 2048);
      }
      if (H0 && QKV) {
#pragma unroll
        for (int e = 0; e < 4; e++) A[0][e] = ones ? 0x3f803f80u : A[0][e];
      }
      u32x4 T = *(const LAS u32x4*)(sb + s0 + ks * 1024);
#pragma unroll
      for (int e = 0; e < 4; e++) { sm[ks] = dot2(T[e], 0x3f803f80u, sm[ks]); sq[ks] = dot2(T[e], T[e], sq[ks]); }
      if (QKV && ks == 0 && !ABL_NS) {
        u32x4 Hh = *(const LAS u32x4*)(sb + sh);
#pragma unroll
        for (int e = 0; e < 4; e++) { sm[2] = dot2(Hh[e], 0x3f803f80u, sm[2]); sq[2] = dot2(Hh[e], Hh[e], sq[2]); }
      }
#pragma unroll
      for (int r = 0; r < NRB; r++)
#pragma unroll
        for (int s = 0; s < 3; s++) {
          if (QKV && s == 2) acc[r][s] = __builtin_amdgcn_mfma_f32_16x16x32_bf16(b8(A[r]), b8(B[s]), acc[r][s], 0, 0, 0);
          else acc[r][s] = __builtin_amdgcn_mfma_f32_16x16x32_bf16(b8(B[s]), b8(A[r]), acc[r][s], 0, 0, 0);
        }
    }
    slot = slot == 2 ? 0 : slot + 1;
  }
  STAMP(2);
#pragma unroll
  for (int i = 0; i < (QKV ? 3 : 2); i++) {
    float a = sm[i], b = sq[i];
#pragma unroll
    for (int d = 1; d < 8; d <<= 1) { a += __shfl_xor(a, d); b += __shfl_xor(b, d); }
    float mean = a * (1.f / 512.f), var = fmaxf(b * (1.f / 512.f) - mean * mean, 0.f), rs = __builtin_amdgcn_rsqf(var + 1e-5f);
    int row = i < 2 ? 16 + 16 * w + 8 * i + (lane >> 3) : 2 * w + ((lane >> 3) & 1);
    if ((lane & 7) == 0) { *(LAS float*)(smem + ST_MEAN + row * 4) = mean; *(LAS float*)(smem + ST_RSTD + row * 4) = rs; }
  }
  if (H0) {
    if (m == 0) {
      *(LAS f32x4*)(smem + ST_CS + (0 * 64 + c * 16 + 4 * g) * 4) = acc[0][0];
      *(LAS f32x4*)(smem + ST_CS + (1 * 64 + c * 16 + 4 * g) * 4) = acc[0][1];
      if (!QKV) *(LAS f32x4*)(smem + ST_CS + (2 * 64 + c * 16 + 4 * g) * 4) = acc[0][2];
    }
    if (QKV) {
      if (g == 0) *(LAS float*)(smem + ST_CS + (2 * 64 + c * 16 + m) * 4) = acc[0][2][0];
      *(LAS f32x4*)(smem + KVX + c * 2048 + lane * 16) = acc[4][1];
      *(LAS f32x4*)(smem + KVX + c * 2048 + 1024 + lane * 16) = acc[4][2];
    }
  } else {
#pragma unroll
    for (int r = 4; r >= 1; r--)
#pragma unroll
      for (int s = 0; s < 3; s++) acc[r][s] = acc[r - 1][s];
  }
  wait_lgkm0();
  bar();
  if (!H0 && QKV) {
    acc[0][1] = *(const LAS f32x4*)(smem + KVX + c * 2048 + lane * 16);
    acc[0][2] = *(const LAS f32x4*)(smem + KVX + c * 2048 + 1024 + lane * 16);
  }
}

__global__ __launch_bounds__(512, 2) void fused_block(Params P) {
  __shared__ __attribute__((aligned(16))) char smem_[163840];
  LAS char* smem = (LAS char*)smem_;
  const unsigned ldsb = (unsigned)(size_t)smem;
  u64 stamps[NSTAMP];
  STAMP(0);
  const int tid = threadIdx.x, lane = tid & 63, wave = __builtin_amdgcn_readfirstlane(tid >> 6);
  const int hf = wave >> 2, c = wave & 3, g = lane >> 4, m = lane & 15;
  const int b = blockIdx.x, xcd = b & 7, r5 = b >> 3;
  const int p = (xcd & 1) * 8 + (r5 & 7), jj = r5 >> 3, j = (xcd >> 1) * 2 + (jj & 1);
  const bool qkv = jj < 2;
  const int rot1 = ROT1_MODE == 0 ? 0 : ROT1_MODE == 1 ? (p + jj) : ROT1_MODE == 2 ? ((p + jj) & 1) * 4 : ROT1_MODE == 4 ? ((p + jj) & 3) * 2 : (p + 2 * jj);
  const int rt = xcd * 4 + jj, ct = r5 & 7;

  B2Ctx b2c;
  b2c.wo = P.wo + ct * 128; b2c.w2 = P.w2 + ct * 128; b2c.hf = hf; b2c.q = c; b2c.ldsb = ldsb;
  b2c.kq0 = c < 3 ? 30 * c : 89; b2c.nq = c < 2 ? 30 : 29;
  b2c.rot = ROT2_MODE == 0 ? 0 : ROT2_MODE == 1 ? ((ct + rt) & 1) * 15 : ROT2_MODE == 3 ? ((ct + rt) % 3) * 10 : ((ct + rt) & 3) * 7;
  b2c.voff = (lane >> 3) * 1024 + (((lane & 7) ^ (((lane >> 4) & 1) << 2)) * 16);

  f32x4 acc[5][3];
  if (qkv) { if (hf == 0) phase1<true, true>(P, smem, ldsb, p, j, c, lane, rot1, b2c, acc, stamps); else phase1<false, true>(P, smem, ldsb, p, j, c, lane, rot1, b2c, acc, stamps); }
  else     { if (hf == 0) phase1<true, false>(P, smem, ldsb, p, j, c, lane, rot1, b2c, acc, stamps); else phase1<false, false>(P, smem, ldsb, p, j, c, lane, rot1, b2c, acc, stamps); }
  STAMP(3);
  u64 e_old;
  asm volatile("s_load_dwordx2 %0, %1, 0x0" : "=s"(e_old) : "s"(P.ws) : "memory");
#pragma unroll
  for (int s = 12; s < 18; s++) b2_issue(b2c, s);

  const __amdgpu_buffer_rsrc_t act = __builtin_amdgcn_make_buffer_rsrc(P.ws + WS_ACT, 0, ACT_BYTES, 0x00020000);
  const int lrow0 = hf * 64;
  const int rt32 = p * 4 + hf * 2;
  if (qkv) {
    const f32x4 csq = *(const LAS f32x4*)(smem + ST_CS + (c * 16 + 4 * g) * 4), csk = *(const LAS f32x4*)(smem + ST_CS + (64 + c * 16 + 4 * g) * 4);
    const float csv = *(const LAS float*)(smem + ST_CS + (128 + c * 16 + m) * 4);
    u32x2 qf[5], kf[5], vf[5];
#pragma unroll
    for (int r = 0; r < 5; r++) {
      const float mu = *(const LAS float*)(smem + ST_MEAN + (lrow0 + 16 * r + m) * 4), rs = *(const LAS float*)(smem + ST_RSTD + (lrow0 + 16 * r + m) * 4);
      const f32x4 mu4 = *(const LAS f32x4*)(smem + ST_MEAN + (lrow0 + 16 * r + 4 * g) * 4), rs4 = *(const LAS f32x4*)(smem + ST_RSTD + (lrow0 + 16 * r + 4 * g) * 4);
      f32x4 k4 = (acc[r][1] - mu * csk) * rs, v4 = (acc[r][2] - mu4 * csv) * rs4;
      kf[r] = (u32x2){pk_bf16(k4[0], k4[1]), pk_bf16(k4[2], k4[3])};
      vf[r] = (u32x2){pk_bf16(v4[0], v4[1]), pk_bf16(v4[2], v4[3])};
      if (r > 0) { f32x4 q4 = (acc[r][0] - mu * csq) * rs; qf[r] = (u32x2){pk_bf16(q4[0], q4[1]), pk_bf16(q4[2], q4[3])}; }
    }
    const bool odd = m & 1;
    const int k0 = 4 * g + (m & 1), k1 = k0 + 2;
    const bool vc0 = k0 <= m, vc1 = k1 <= m, vp0 = k0 >= m + 2, vp1 = k1 >= m + 2;
    const unsigned pm = odd ? 0xffff0000u : 0x0000ffffu;
    const float SC = 0.25f * 1.4426950408889634f, NEG = -3.0e38f;
    const u32x4 one4 = {0x3f803f80u, 0x3f803f80u, 0x3f803f80u, 0x3f803f80u};
    u32x2 of[5];
#pragma unroll
    for (int r = 1; r < 5; r++) {
      const f32x4 z = {0.f, 0.f, 0.f, 0.f};
      f32x4 sp = __builtin_amdgcn_mfma_f32_16x16x16bf16_1k(__builtin_bit_cast(s16x4, kf[r - 1]), __builtin_bit_cast(s16x4, qf[r]), z, 0, 0, 0);
      f32x4 sc = __builtin_amdgcn_mfma_f32_16x16x16bf16_1k(__builtin_bit_cast(s16x4, kf[r]), __builtin_bit_cast(s16x4, qf[r]), z, 0, 0, 0);
      const bool prev_ok = !(r == 1 && hf == 0 && p == 0);
      float p0 = odd ? sp[1] : sp[0], p1 = odd ? sp[3] : sp[2], c0 = odd ? sc[1] : sc[0], c1 = odd ? sc[3] : sc[2];
      p0 = (vp0 && prev_ok) ? p0 : NEG; p1 = (vp1 && prev_ok) ? p1 : NEG; c0 = vc0 ? c0 : NEG; c1 = vc1 ? c1 : NEG;
      const float mx = xmax16_32(fmaxf(fmaxf(p0, p1), fmaxf(c0, c1))) * SC;
      p0 = __builtin_amdgcn_exp2f(p0 * SC - mx); p1 = __builtin_amdgcn_exp2f(p1 * SC - mx);
      c0 = __builtin_amdgcn_exp2f(c0 * SC - mx); c1 = __builtin_amdgcn_exp2f(c1 * SC - mx);
      const u32x4 pf = {pk_bf16(p0, p0) & pm, pk_bf16(p1, p1) & pm, pk_bf16(c0, c0) & pm, pk_bf16(c1, c1) & pm};
      const u32x4 vv = {vf[r - 1].x, vf[r - 1].y, vf[r].x, vf[r].y};
      f32x4 o = __builtin_amdgcn_mfma_f32_16x16x32_bf16(b8(vv), b8(pf), z, 0, 0, 0);
      f32x4 l = __builtin_amdgcn_mfma_f32_16x16x32_bf16(b8(one4), b8(pf), z, 0, 0, 0);
      const float inv = __builtin_amdgcn_rcpf(l[0]);
      of[r] = (u32x2){pk_bf16(o[0] * inv, o[1] * inv), pk_bf16(o[2] * inv, o[3] * inv)};
    }
#pragma unroll
    for (int pr = 0; pr < 2; pr++) {
      auto s0 = __builtin_amdgcn_permlane16_swap(of[1 + 2 * pr].x, of[2 + 2 * pr].x, false, false);
      auto s1 = __builtin_amdgcn_permlane16_swap(of[1 + 2 * pr].y, of[2 + 2 * pr].y, false, false);
      __builtin_amdgcn_raw_buffer_store_b128((u32x4){s0[0], s1[0], s0[1], s1[1]}, act, lane * 16 + ((rt32 + pr) * NK16 + 4 * j + c) * 1024, 0, 16);
    }
  } else {
    const int nblk = (j == 7) ? 9 : 11;
    float mu[4], rs[4];
#pragma unroll
    for (int r = 0; r < 4; r++) { mu[r] = *(const LAS float*)(smem + ST_MEAN + (lrow0 + 16 + 16 * r + m) * 4); rs[r] = *(const LAS float*)(smem + ST_RSTD + (lrow0 + 16 + 16 * r + m) * 4); }
#pragma unroll
    for (int s = 0; s < 3; s++) {
      if (4 * s + c < nblk) {
        const f32x4 cs = *(const LAS f32x4*)(smem + ST_CS + (s * 64 + c * 16 + 4 * g) * 4);
        const u32x2 b1v = *(const LAS u32x2*)(smem + KVX + ((4 * s + c) * 16 + 4 * g) * 2);
        const f32x4 bb = {bf_lo(b1v.x), bf_hi(b1v.x), bf_lo(b1v.y), bf_hi(b1v.y)};
        u32x2 hfr[4];
#pragma unroll
        for (int r = 0; r < 4; r++) {
          f32x4 v = (acc[r + 1][s] - mu[r] * cs) * rs[r] + bb;
          f32x4 zz = v * (v * v * (-0.10294324f) + (-2.3022082f));
          f32x4 h;
#pragma unroll
          for (int e = 0; e < 4; e++) h[e] = v[e] * __builtin_amdgcn_rcpf(1.f + __builtin_amdgcn_exp2f(zz[e]));
          hfr[r] = (u32x2){pk_bf16(h[0], h[1]), pk_bf16(h[2], h[3])};
        }
#pragma unroll
        for (int pr = 0; pr < 2; pr++) {
          auto s0 = __builtin_amdgcn_permlane16_swap(hfr[2 * pr].x, hfr[2 * pr + 1].x, false, false);
          auto s1 = __builtin_amdgcn_permlane16_swap(hfr[2 * pr].y, hfr[2 * pr + 1].y, false, false);
          __builtin_amdgcn_raw_buffer_store_b128((u32x4){s0[0], s1[0], s0[1], s1[1]}, act, lane * 16 + ((rt32 + pr) * NK16 + 32 + 11 * j + 4 * s + c) * 1024, 0, 16);
        }
      }
    }
  }
  STAMP(4);
  asm volatile("s_waitcnt vmcnt(0) lgkmcnt(0)" : "+s"(e_old) :: "memory");
  bar();
  const u64 tag = mix64(e_old);
  if (tid == 0) __hip_atomic_store((GAS u64*)(P.ws + WS_FLAG + p * 128 + (qkv ? j : 8 + j) * 8), tag, __ATOMIC_RELAXED, __HIP_MEMORY_SCOPE_AGENT);
  STAMP(5);
  const int q4 = m >> 2, pp = lane & 3;
  const int tb0 = c * 2048 + (8 * (g >> 1) + q4) * 128 + (((2 * (g & 1) + (pp >> 1)) ^ ((q4 >> 1) << 2)) * 16) + (pp & 1) * 8;
  u32x4 BR[10][2];
#pragma unroll
  for (int s = 0; s < 10; s++)
#pragma unroll
    for (int cb = 0; cb < 2; cb++) {
      u32x2 lo = trd(smem + b2grp(s) * 8192 + (tb0 ^ (cb * 64))), hi = trd(smem + b2grp(s) * 8192 + (tb0 ^ (cb * 64)) + 512);
      BR[s][cb] = (u32x4){lo.x, lo.y, hi.x, hi.y};
    }
  wait_lgkm0();
  bar();
#pragma unroll
  for (int s = 18; s < 30; s++) b2_issue(b2c, s);
  const int orow = rt * 64 + wave * 8 + (lane >> 3), ocol = ct * 64 + (lane & 7) * 8;
  const u32x4 xres = *(const GAS u32x4*)(P.x + orow * 1024 + ocol * 2);
  const u32x4 b2v = *(const GAS u32x4*)(P.b2 + ocol * 2);
  if (wave == 0) {
    const GAS u64* fl = (const GAS u64*)(P.ws + WS_FLAG + (rt >> 1) * 128 + (lane & 15) * 8);
    for (unsigned spin = 0; spin < SPIN_CAP; spin++) {
      u64 v = __hip_atomic_load(fl, __ATOMIC_RELAXED, __HIP_MEMORY_SCOPE_AGENT);
      if (__all(v == tag)) break;
      __builtin_amdgcn_s_sleep(1);
    }
  }
  bar();
  STAMP(6);
  f32x16 o2[2];
#pragma unroll
  for (int e = 0; e < 16; e++) { o2[0][e] = 0.f; o2[1][e] = 0.f; }
  constexpr int PF = 8;
  u32x4 AF[PF];
  const int abase = (rt * 2 + hf) * NK16;
  auto aload = [&](int s) -> u32x4 {
    const int vo = (s == 29 && b2c.nq == 29) ? 0x7f000000 : lane * 16;
    return __builtin_amdgcn_raw_buffer_load_b128(act, vo, (abase + phys_k16(b2c, s)) * 1024, 16);
  };
#pragma unroll
  for (int s = 0; s < PF; s++) AF[s] = aload(s);
#pragma unroll
  for (int s = 0; s < 30; s++) {
    if (s == 18) { wait_lgkm0(); bar(); }
    u32x4 B0, B1;
    if (s < 10) { B0 = BR[s][0]; B1 = BR[s][1]; }
    else {
      u32x2 lo = trd(smem + b2grp(s) * 8192 + tb0), hi = trd(smem + b2grp(s) * 8192 + tb0 + 512);
      B0 = (u32x4){lo.x, lo.y, hi.x, hi.y};
      lo = trd(smem + b2grp(s) * 8192 + (tb0 ^ 64)); hi = trd(smem + b2grp(s) * 8192 + (tb0 ^ 64) + 512);
      B1 = (u32x4){lo.x, lo.y, hi.x, hi.y};
    }
    const u32x4 a = AF[s % PF];
    o2[0] = __builtin_amdgcn_mfma_f32_32x32x16_bf16(b8(B0), b8(a), o2[0], 0, 0, 0);
    o2[1] = __builtin_amdgcn_mfma_f32_32x32x16_bf16(b8(B1), b8(a), o2[1], 0, 0, 0);
    if (s + PF < 30) AF[s % PF] = aload(s + PF);
    __builtin_amdgcn_sched_barrier(0);
  }
  STAMP(7);
  wait_lgkm0();
  bar();
  {
    LAS char* wp = smem + c * 17408 + (hf * 32 + (lane & 31)) * 272 + (lane >> 5) * 16;
#pragma unroll
    for (int cb = 0; cb < 2; cb++)
#pragma unroll
      for (int rq = 0; rq < 4; rq++)
        *(LAS f32x4*)(wp + cb * 128 + rq * 32) = (f32x4){o2[cb][4 * rq], o2[cb][4 * rq + 1], o2[cb][4 * rq + 2], o2[cb][4 * rq + 3]};
  }
  wait_lgkm0();
  bar();
  {
    const LAS char* rp = smem + (wave * 8 + (lane >> 3)) * 272 + (lane & 7) * 32;
    f32x4 u0 = {bf_lo(xres.x) + bf_lo(b2v.x), bf_hi(xres.x) + bf_hi(b2v.x), bf_lo(xres.y) + bf_lo(b2v.y), bf_hi(xres.y) + bf_hi(b2v.y)};
    f32x4 u1 = {bf_lo(xres.z) + bf_lo(b2v.z), bf_hi(xres.z) + bf_hi(b2v.z), bf_lo(xres.w) + bf_lo(b2v.w), bf_hi(xres.w) + bf_hi(b2v.w)};
#pragma unroll
    for (int q = 0; q < 4; q++) { u0 += *(const LAS f32x4*)(rp + q * 17408); u1 += *(const LAS f32x4*)(rp + q * 17408 + 16); }
    *(GAS u32x4*)(P.out + orow * 1024 + ocol * 2) = (u32x4){pk_bf16(u0[0], u0[1]), pk_bf16(u0[2], u0[3]), pk_bf16(u1[0], u1[1]), pk_bf16(u1[2], u1[3])};
  }
  if (b == 0 && wave == 0) {
    const GAS u64* fl = (const GAS u64*)(P.ws + WS_FLAG) + lane;
    for (unsigned spin = 0; spin < SPIN_CAP; spin++) {
      u64 v0 = __hip_atomic_load(fl, __ATOMIC_RELAXED, __HIP_MEMORY_SCOPE_AGENT), v1 = __hip_atomic_load(fl + 64, __ATOMIC_RELAXED, __HIP_MEMORY_SCOPE_AGENT);
      u64 v2 = __hip_atomic_load(fl + 128, __ATOMIC_RELAXED, __HIP_MEMORY_SCOPE_AGENT), v3 = __hip_atomic_load(fl + 192, __ATOMIC_RELAXED, __HIP_MEMORY_SCOPE_AGENT);
      if (__all(v0 == tag && v1 == tag && v2 == tag && v3 == tag)) break;
      __builtin_amdgcn_s_sleep(1);
    }
    if (lane == 0) __hip_atomic_store((GAS u64*)P.ws, tag, __ATOMIC_RELAXED, __HIP_MEMORY_SCOPE_AGENT);
  }
  STAMP(8);
  { u64 pv = *(const GAS u64*)(P.ws + 512); u64 until = stamps[8] + PREAMP * (stamps[0] - pv); while (__builtin_amdgcn_s_memrealtime() < until) __builtin_amdgcn_s_sleep(2); }
  if (DIAG) { if (tid == 0) for (int i = 0; i < NSTAMP; i++) ((GAS u64*)(P.ws + WS_DIAG))[blockIdx.x * 16 + i] = stamps[i]; }
}

__global__ void pre_k(char* ws) { if (threadIdx.x == 0 && blockIdx.x == 0) *(GAS u64*)(ws + 512) = __builtin_amdgcn_s_memrealtime();
  asm volatile("buffer_wbl2 sc0 sc1\n\ts_waitcnt vmcnt(0)" ::: "memory");
}
#define PLDS 64
#define PTHR 256
#define PGRID 1
__global__ __launch_bounds__(PTHR) void post_k(char* ws, int amp) {
  u64 now = __builtin_amdgcn_s_memrealtime();
  __shared__ u64 sm[PLDS / 8];
  u64 v = ((const GAS u64*)(ws + WS_DIAG))[(threadIdx.x & 255) * 16 + 8];
  for (int d = 1; d < 64; d <<= 1) { u64 o = __shfl_xor(v, d); v = o > v ? o : v; }
  if ((threadIdx.x & 63) == 0) sm[(threadIdx.x >> 6) & 3] = v; if (amp == 77) sm[threadIdx.x + PLDS / 8 - 1024] = v; __syncthreads();
  v = sm[0]; for (int i = 1; i < 4; i++) v = sm[i] > v ? sm[i] : v;
  u64 until = now + amp * (now - v); while (__builtin_amdgcn_s_memrealtime() < until) __builtin_amdgcn_s_sleep(2);
}
extern "C" void kernel_launch(void* const* d_in, const int* in_sizes, int n_in, void* d_out, int out_size, void* d_ws, size_t ws_size,
                              hipStream_t stream) {
  Params P;
  P.x = (const char*)d_in[0]; P.wqkv = (const char*)d_in[1]; P.wo = (const char*)d_in[2];
  P.w1 = (const char*)d_in[4]; P.b1 = (const char*)d_in[5]; P.w2 = (const char*)d_in[6]; P.b2 = (const char*)d_in[7];
  P.out = (char*)d_out; P.ws = (char*)d_ws;
  pre_k<<<PREGRID, 64, 0, stream>>>(P.ws);
  fused_block<<<256, 512, 0, stream>>>(P);
}
```

```cpp
#define USEPF
#define PFMODE 1
#define ROT2_MODE 0
#include <hip/hip_runtime.h>
#include <stdint.h>

typedef __attribute__((ext_vector_type(8))) __bf16 bf16x8;
typedef __attribute__((ext_vector_type(2))) __bf16 bf16x2;
typedef __attribute__((ext_vector_type(4))) short s16x4;
typedef __attribute__((ext_vector_type(4))) float f32x4;
typedef __attribute__((ext_vector_type(2))) float f32x2;
typedef __attribute__((ext_vector_type(16))) float f32x16;
typedef __attribute__((ext_vector_type(4))) unsigned u32x4;
typedef __attribute__((ext_vector_type(2))) unsigned u32x2;
#define LAS __attribute__((address_space(3)))
#define GAS __attribute__((address_space(1)))
typedef unsigned long long u64;

#define DIAG 0
#define ABL_NS 0
#define PF 5
#define BG 3
#define PFCH(w) (1 + ((w) >> 1))
#define RPG 1
#define ROT1_MODE 1

constexpr int NK16 = 118;
constexpr int SLOT = 43008, XB = 18432;
constexpr int ST_MEAN = 129024, ST_RSTD = ST_MEAN + 576, ST_CS = ST_MEAN + 1152;
constexpr int PAIRF = ST_MEAN + 1920;
constexpr int KVX = 19 * 8192;
constexpr int WS_FLAG = 1024, WS_DIAG = 32768, WS_STAT = 65536, WS_ACT = 131072;
constexpr int ACT_BYTES = 64 * NK16 * 1024;
constexpr int NSTAMP = 9;
constexpr unsigned SPIN_CAP = 1u << 20;

struct Params {
  const char* x; const char* wqkv; const char* wo; const char* w1; const char* b1; const char* w2; const char* b2;
  char* out; char* ws;
};

__device__ __forceinline__ void glds16(unsigned voff, const char* sbase, unsigned lds) {
  asm volatile("s_mov_b32 m0, %2\n\ts_nop 0\n\tglobal_load_lds_dwordx4 %0, %1" :: "v"(voff), "s"(sbase), "s"(lds) : "memory");
}
__device__ __forceinline__ void glds16_mask(unsigned voff, const char* sbase, unsigned lds, u64 mask) {
  asm volatile("s_mov_b32 m0, %2\n\ts_mov_b64 exec, %3\n\tglobal_load_lds_dwordx4 %0, %1\n\ts_mov_b64 exec, -1" :: "v"(voff), "s"(sbase), "s"(lds), "s"(mask) : "memory");
}
__device__ __forceinline__ void set_m0(unsigned base, int add) { asm volatile("s_add_i32 m0, %0, %1" :: "s"(base), "i"(add) : "memory", "scc"); }
template <int IMM> __device__ __forceinline__ void glds16i(unsigned voff, const char* sbase) {
  asm volatile("global_load_lds_dwordx4 %0, %1 offset:%2" :: "v"(voff), "s"(sbase), "i"(IMM) : "memory");
}
template <int IMM> __device__ __forceinline__ void glds16i_mask(unsigned voff, const char* sbase, u64 mask) {
  asm volatile("s_mov_b64 exec, %3\n\tglobal_load_lds_dwordx4 %0, %1 offset:%2\n\ts_mov_b64 exec, -1" :: "v"(voff), "s"(sbase), "i"(IMM), "s"(mask) : "memory");
}
template <int N> __device__ __forceinline__ void wait_vm() { asm volatile("s_waitcnt vmcnt(%0) lgkmcnt(0)" :: "i"(N) : "memory"); }
__device__ __forceinline__ void wait_lgkm0() { asm volatile("s_waitcnt lgkmcnt(0)" ::: "memory"); }
__device__ __forceinline__ void bar() { asm volatile("s_barrier" ::: "memory"); }
__device__ __forceinline__ unsigned pk_bf16(float a, float b) { f32x2 p = {a, b}; return __builtin_bit_cast(unsigned, __builtin_convertvector(p, bf16x2)); }
__device__ __forceinline__ float dot2(unsigned a, unsigned b, float c) { return __builtin_amdgcn_fdot2_f32_bf16(__builtin_bit_cast(bf16x2, a), __builtin_bit_cast(bf16x2, b), c, false); }
__device__ __forceinline__ u32x2 trd(const LAS char* p) { return __builtin_bit_cast(u32x2, __builtin_amdgcn_ds_read_tr16_b64_v4i16((LAS s16x4*)p)); }
__device__ __forceinline__ bf16x8 b8(u32x4 v) { return __builtin_bit_cast(bf16x8, v); }
__device__ __forceinline__ float bf_lo(unsigned v) { return __uint_as_float(v << 16); }
__device__ __forceinline__ float bf_hi(unsigned v) { return __uint_as_float(v & 0xffff0000u); }
__device__ __forceinline__ float xmax16_32(float v) {
  auto a = __builtin_amdgcn_permlane16_swap(__float_as_uint(v), __float_as_uint(v), false, false);
  v = fmaxf(__uint_as_float(a[0]), __uint_as_float(a[1]));
  auto b = __builtin_amdgcn_permlane32_swap(__float_as_uint(v), __float_as_uint(v), false, false);
  return fmaxf(__uint_as_float(b[0]), __uint_as_float(b[1]));
}
__device__ __forceinline__ u64 mix64(u64 z) {
  z += 0x9E3779B97F4A7C15ull; z = (z ^ (z >> 30)) * 0xBF58476D1CE4E5B9ull; z = (z ^ (z >> 27)) * 0x94D049BB133111EBull; return z ^ (z >> 31);
}
#define STAMP(i) do { if (DIAG) { asm volatile("s_memrealtime %0\n\ts_waitcnt lgkmcnt(0)" : "=s"(stamps[i]) :: "memory"); } } while (0)

#define TS(i)
#define TSD(i)
constexpr int WS_TS = 16 * 1024 * 1024;
__device__ __forceinline__ constexpr int b2grp(int s) {
  return s < 3 ? 16 + s : s < 7 ? 8 + s : s < 18 ? s - 7 : s < 21 ? s - 2 : s < 25 ? s - 10 : s < 28 ? s - 25 : s == 28 ? 15 : 19;
}

struct B2Ctx { const char* wo; const char* w2; int kq0, nq, rot; unsigned voff, voff1, vhalf, mhalf, mboth; };
__device__ __forceinline__ int phys_k16(const B2Ctx& c, int s) { int t = s + c.rot; t = t >= c.nq ? t - c.nq : t; t = (s >= c.nq) ? 0 : t; return c.kq0 + t; }
__device__ __forceinline__ const char* b2_src(const B2Ctx& c, int s) {
  int row0 = phys_k16(c, s) * 16;
  return row0 < 512 ? c.wo + row0 * 1024 : c.w2 + (row0 - 512) * 1024;
}
__device__ __forceinline__ void b2_issue(const B2Ctx& c, int s) {
  const char* src = b2_src(c, s);
  set_m0(c.mhalf, b2grp(s) * 8192); asm volatile("s_nop 0");
  glds16i<0>(c.vhalf, src);
}
__device__ __forceinline__ void b2_issue2(const B2Ctx& c, int s) {
  const char* src = b2_src(c, s);
  set_m0(c.mboth, b2grp(s) * 8192); asm volatile("s_nop 0");
  glds16i<0>(c.voff, src); glds16i<1024>(c.voff1, src);
}

#define SB() __builtin_amdgcn_sched_barrier(0)
struct BRegs { u32x2 lo[10][2], hi[10][2]; };
__device__ __forceinline__ void br_read(BRegs& BR, const LAS char* smem, int tb0, int i) {
  const int s = i >> 2, cb = (i >> 1) & 1;
  if (i & 1) BR.hi[s][cb] = trd(smem + b2grp(s) * 8192 + (tb0 ^ (cb * 64)) + 512); else BR.lo[s][cb] = trd(smem + b2grp(s) * 8192 + (tb0 ^ (cb * 64)));
}
__device__ __forceinline__ float max3f(float a, float b, float c) { return fmaxf(fmaxf(a, b), c); }
template <int CTRL> __device__ __forceinline__ float dpp_add(float v) {
  return v + __builtin_bit_cast(float, __builtin_amdgcn_update_dpp(0, __builtin_bit_cast(int, v), CTRL, 0xf, 0xf, true));
}
__device__ __forceinline__ float half_sum(float v) {
  v = dpp_add<0xB1>(v); v = dpp_add<0x4E>(v); v = dpp_add<0x141>(v); v = dpp_add<0x140>(v);
  auto a = __builtin_amdgcn_permlane16_swap(__float_as_uint(v), __float_as_uint(v), false, false);
  return __uint_as_float(a[0]) + __uint_as_float(a[1]);
}

template <bool H0, bool QKV>
struct P1 {
  static constexpr int NRB = H0 ? 5 : 4;
  static constexpr int NL = H0 ? (QKV ? 5 : 4) : 6;
  struct Frag { u32x4 A[NRB]; u32x2 Bl[3], Bh[3]; };
  unsigned mbase, mbaseh; u64 hmask;
  unsigned vo[6]; const char* cb[8];
  int hdelta;
  unsigned pb, pa0, pa1;
  u32x4 ones;

  template <int I> __device__ __forceinline__ void ld(int ch, int slot, bool m0) const {
    if (I >= NL) return;
    if (H0 && I == 4) { set_m0(mbaseh, slot * SLOT); glds16i_mask<0>(vo[4], cb[ch] + hdelta, hmask); return; }
    if (m0) { set_m0(mbase, slot * SLOT); asm volatile("s_nop 0"); }
    glds16i<(H0 ? I * 1024 : I * 1024 - 3072)>(vo[I], cb[ch]);
  }
  __device__ __forceinline__ void load1(int ch, int slot, int i, bool m0) const {
    if (i == 0) ld<0>(ch, slot, m0); else if (i == 1) ld<1>(ch, slot, m0); else if (i == 2) ld<2>(ch, slot, m0);
    else if (i == 3) ld<3>(ch, slot, m0); else if (i == 4) ld<4>(ch, slot, m0); else ld<5>(ch, slot, m0);
  }
  __device__ __forceinline__ void rd(Frag& f, int ks, int n) const {
    constexpr int ord[11] = {0, 1, 6, 2, 3, 4, 5, 7, 8, 9, 10};
    if (n >= 11) return;
    const int i = ord[n];
    if (i < 6) { const LAS char* q = (const LAS char*)(size_t)pb; const int s = i >> 1; if (i & 1) f.Bh[s] = trd(q + (32 * ks + 4) * 384 + s * 128); else f.Bl[s] = trd(q + (32 * ks) * 384 + s * 128); }
    else if (i - 6 < NRB) {
      const int r = H0 ? (i - 5) % 5 : i - 6;
      if (!(H0 && !QKV && r == 0)) f.A[r] = *(const LAS u32x4*)((const LAS char*)(size_t)(ks ? pa1 : pa0) + r * 2048);
    }
  }
  __device__ __forceinline__ void advance(int delta) {
    asm volatile("v_add_u32 %0, %3, %0\n\tv_add_u32 %1, %3, %1\n\tv_add_u32 %2, %3, %2" : "+v"(pb), "+v"(pa0), "+v"(pa1) : "i"(delta));
  }
  template <class F> __device__ __forceinline__ void mma(f32x4 (&acc)[5][3], Frag& f, F&& fill) const {
#pragma unroll
    for (int rr = 0; rr < NRB; rr++) {
      const int r = H0 ? (rr + 1) % 5 : rr;
#pragma unroll
      for (int s = 0; s < 3; s++) {
        const u32x4 B = {f.Bl[s].x, f.Bl[s].y, f.Bh[s].x, f.Bh[s].y};
        const u32x4 A = (H0 && !QKV && r == 0) ? ones : f.A[r];
        if (QKV && s == 2) acc[r][s] = __builtin_amdgcn_mfma_f32_16x16x32_bf16(b8(A), b8(B), acc[r][s], 0, 0, 0);
        else acc[r][s] = __builtin_amdgcn_mfma_f32_16x16x32_bf16(b8(B), b8(A), acc[r][s], 0, 0, 0);
        SB();
        fill(3 * rr + s);
        SB();
      }
    }
  }
  template <class F> __device__ __forceinline__ void stepA(f32x4 (&acc)[5][3], Frag& cur, Frag& nx, int delta, F&& site) {
    mma(acc, cur, [&](int n) {
      rd(nx, 1, n);
      constexpr int S0 = H0 ? 4 : 2, SD = H0 ? 4 : 4;
      if (n >= S0 && (n - S0) % SD == 0 && (n - S0) / SD < 3) site((n - S0) / SD);
      if (n == 3 * NRB - 2 && delta != 0) advance(delta);
    });
  }
  template <class S, class F> __device__ __forceinline__ void stepB(f32x4 (&acc)[5][3], Frag& cur, Frag& nx, S&& sync, F&& site) const {
    mma(acc, cur, [&](int n) {
      if (n == BG - 1) sync();
      if (n >= BG - 1) { const int i = n - (BG - 1); if (i < 2) { rd(nx, 0, 2 * i); rd(nx, 0, 2 * i + 1); } else rd(nx, 0, i + 2); }
      constexpr int S0 = H0 ? 6 : 5, SD = H0 ? 4 : 3;
      if (n >= S0 && (n - S0) % SD == 0 && (n - S0) / SD < 3) site((n - S0) / SD);
    });
  }
};

template <bool H0, bool QKV>
__device__ __forceinline__ void phase1(const Params& P, LAS char* smem, unsigned ldsb, int p, int j, int c, int lane, int rot, const B2Ctx& b2c,
                                       f32x4 (&acc)[5][3], u64& tag, u64 (&stamps)[NSTAMP], unsigned& tsv) {
  typedef P1<H0, QKV> T;
  constexpr int NL = T::NL, NS = H0 ? 1 : 0;
  const int g = lane >> 4, m = lane & 15, w = (H0 ? 0 : 4) + c;
  T t;
  const int srow = p * 128 + ((QKV ? 0 : 8) + j) * 8 + 2 * c;
  u32x4 xr0, xr1;
  const char* base0 = (H0 ? P.x + p * 131072 : QKV ? P.wqkv + j * 128 : P.w1 + j * 352) - 4096;
  constexpr int kstride = H0 ? 128 : QKV ? 64 * 3072 : 64 * 2752;
  t.cb[0] = base0 + (rot & 7) * kstride;
  if (H0) {
    t.mbase = ldsb + (16 + 32 * c) * 128; t.mbaseh = ldsb + 4 * c * 128;
    t.hdelta = p > 0 ? -16384 : 0;
    t.hmask = c == 0 ? 0xffff0000ull : 0xffffffffull;
#pragma unroll
    for (int i = 0; i < 4; i++) { t.vo[i] = 4096 - 1024 * i + (32 * c + 8 * i + (lane >> 3)) * 1024 + (((lane & 7) ^ (4 * (i & 1) + (lane >> 4))) * 16); t.load1(0, 0, i, i == 0); }
    { int row = 4 * c + ((lane >> 3) & 3); t.vo[4] = 4096 + row * 1024 + (((lane & 7) ^ (row >> 1)) * 16); t.load1(0, 0, 4, true); }
    asm volatile("global_load_dwordx4 %0, %2, %3\n\tglobal_load_dwordx4 %1, %2, %3 offset:512" : "=&v"(xr0), "=&v"(xr1) : "v"((lane >> 5) * 1024 + (lane & 31) * 16), "s"(P.x + srow * 1024) : "memory");
  } else {
    t.mbase = ldsb + XB + c * 6144 + 3072;
    const int nval = (j == 7) ? 18 : 22;
#pragma unroll
    for (int i = 0; i < 6; i++) {
      int u = (c * 6 + i) * 64 + lane, row = u / 24, ch = u - row * 24, sec = ch >> 3;
      int cc = (ch & 7) ^ (2 * (((row >> 1) & 1) | ((row >> 2) & 2)));
      if (QKV) t.vo[i] = 4096 + 3072 - 1024 * i + row * 3072 + sec * 1024 + cc * 16;
      else { int c16 = sec * 8 + cc; c16 = c16 < nval ? c16 : nval - 1; t.vo[i] = 4096 + 3072 - 1024 * i + row * 2752 + c16 * 16; }
      t.load1(0, 0, i, i == 0);
    }
  }
  u64 e_old;
  asm volatile("s_load_dwordx2 %0, %1, 0x0" : "=s"(e_old) : "s"(P.ws) : "memory");
#pragma unroll
  for (int ch = 1; ch < 8; ch++) { t.cb[ch] = base0 + ((ch + rot) & 7) * kstride; asm volatile("" : "+s"(t.cb[ch])); }
  if (!H0) {
#pragma unroll
    for (int i = 0; i < 6; i++) t.load1(1, 1, i, i == 0);
  }
  t.pa0 = ldsb + (H0 ? 0 : 5 * 2048) + m * 128 + ((g ^ (m >> 1)) * 16); t.pa1 = t.pa0 ^ 64;
  { const int q4 = m >> 2, pp = lane & 3;
    t.pb = ldsb + XB + (8 * g + q4) * 384 + (((2 * c + (pp >> 1)) ^ (2 * ((q4 >> 1) | ((g & 1) << 1)))) * 16) + (pp & 1) * 8; }
  { const unsigned o = m == 0 ? 0x3f803f80u : 0u; t.ones = (u32x4){o, o, o, o}; if (H0 && !QKV) asm volatile("" : "+v"(t.ones)); }
  if (H0 && QKV) { if (c == 0 && lane < 48) *(LAS u32x4*)(smem + (lane >> 4) * SLOT + m * 16) = (u32x4){0x3f803f80u, 0x3f803f80u, 0x3f803f80u, 0x3f803f80u}; }
  TS(1);
  if (H0) {
    asm volatile("s_waitcnt vmcnt(0) lgkmcnt(0)" : "+v"(xr0), "+v"(xr1), "+s"(e_old) :: "memory");
    TS(2);
    { u64 z = mix64(e_old); unsigned lo = __builtin_amdgcn_readfirstlane((unsigned)z), hi = __builtin_amdgcn_readfirstlane((unsigned)(z >> 32)); asm volatile("" : "+s"(lo), "+s"(hi)); tag = ((u64)hi << 32) | lo; }
    float v[16];
#pragma unroll
    for (int e = 0; e < 4; e++) { v[2 * e] = bf_lo(xr0[e]); v[2 * e + 1] = bf_hi(xr0[e]); v[8 + 2 * e] = bf_lo(xr1[e]); v[9 + 2 * e] = bf_hi(xr1[e]); }
    float s0 = 0.f, s1 = 0.f;
#pragma unroll
    for (int e = 0; e < 8; e++) { s0 += v[e]; s1 += v[8 + e]; }
    const float mean = half_sum(s0 + s1) * (1.f / 512.f);
    float q0 = 0.f, q1 = 0.f;
#pragma unroll
    for (int e = 0; e < 8; e++) { float d = v[e] - mean, d2 = v[8 + e] - mean; q0 = fmaf(d, d, q0); q1 = fmaf(d2, d2, q1); }
    const float rs = __builtin_amdgcn_rsqf(half_sum(q0 + q1) * (1.f / 512.f) + 1e-5f);
    if ((lane & 31) < 2)
      __hip_atomic_store((GAS u64*)(P.ws + WS_STAT + (srow + (lane >> 5)) * 16 + (lane & 1) * 8), (tag << 32) | __float_as_uint((lane & 1) ? rs : mean), __ATOMIC_RELAXED, __HIP_MEMORY_SCOPE_AGENT);
    TS(30);
#pragma unroll
    for (int i = 0; i < 5; i++) t.load1(1, 1, i, i == 0);
    if (!QKV) { if (c == 0) { int l = lane < (j == 7 ? 18 : 22) ? lane : 0; glds16(l * 16, P.b1 + j * 352, ldsb + KVX); } }
    if (lane == 0) *(LAS unsigned*)(smem + PAIRF + c * 4) = 0u;
  } else {
    asm volatile("s_waitcnt lgkmcnt(0)" : "+s"(e_old) :: "memory");
    { u64 z = mix64(e_old); unsigned lo = __builtin_amdgcn_readfirstlane((unsigned)z), hi = __builtin_amdgcn_readfirstlane((unsigned)(z >> 32)); asm volatile("" : "+s"(lo), "+s"(hi)); tag = ((u64)hi << 32) | lo; }
  }
#pragma unroll
  for (int r = 0; r < 5; r++)
#pragma unroll
    for (int s = 0; s < 3; s++) acc[r][s] = (f32x4){0.f, 0.f, 0.f, 0.f};
  typename T::Frag F0, F1;
  unsigned pfd = 0;
  const char* pfa;
  { int l = w * 8 + (lane & 7); int line = blockIdx.x * 59 + (l < 59 ? l : 58); pfa = line < 4096 ? P.wo + line * 128 : P.w2 + (line - 4096) * 128; }
  const int sti = 18 * w + (lane < 18 ? lane : 17);
  unsigned sto; { int row = p * 128 - 16 + sti; sto = (row < 0 ? 0 : row) * 16; asm volatile("" : "+v"(sto), "+v"(pfa)); }
  auto warm = [&](int k) {
    asm volatile("s_mov_b64 exec, %2\n\tglobal_load_dword %0, %1, off\n\ts_mov_b64 exec, -1" : "+v"(pfd) : "v"(pfa), "s"(PFMODE == 1 ? 0x3ull << (2 * k) : 0xffffull << (16 * k)) : "memory");
  };
  constexpr int NP = 1;
  const char* bs[12];
#pragma unroll
  for (int i = 0; i < 12; i++) { bs[i] = b2_src(b2c, i); asm volatile("" : "+s"(bs[i])); }
  auto b2_early = [&](int i) { set_m0(b2c.mhalf, b2grp(i) * 8192); asm volatile("s_nop 0"); glds16i<0>(b2c.vhalf, bs[i]); };
  TS(3);
  if (!H0) wait_vm<NL>(); else wait_lgkm0();
  TS(4);
  bar();
  TS(5);
  STAMP(1);
#pragma unroll
  for (int n = 0; n < 11; n++) t.rd(F0, 0, n);
  t.stepA(acc, F0, F1, SLOT, [&](int k) { t.load1(2, 2, 2 * k, k == 0); t.load1(2, 2, 2 * k + 1, false); });
  t.stepB(acc, F1, F0, [&]() { TS(6); wait_vm<NL>(); TS(7); bar(); TS(8); }, [&](int k) { t.load1(3, 0, k, k == 0); });
#pragma unroll
  for (int ch = 1; ch < 5; ch++) {
    t.stepA(acc, F0, F1, ch % 3 == 2 ? -2 * SLOT : SLOT, [&](int k) { t.load1(ch + 2, (ch + 2) % 3, 3 + k, DIAG == 3 && k == 0); if (k == 2) warm(ch - 1); });
    t.stepB(acc, F1, F0, [&]() { TS(6 + 3 * ch); wait_vm<NL + NP>(); TS(7 + 3 * ch); bar(); TS(8 + 3 * ch); }, [&](int k) { t.load1(ch + 3, ch % 3, k, k == 0); });
  }
  u32x4 st;
  t.stepA(acc, F0, F1, -2 * SLOT, [&](int k) { t.load1(7, 1, 3 + k, DIAG == 3 && k == 0); });
  t.stepB(acc, F1, F0, [&]() {
    TS(21); asm volatile("s_waitcnt vmcnt(%1) lgkmcnt(0)" : "+v"(pfd) : "i"(NL) : "memory"); TS(22); bar(); TS(23);
    asm volatile("global_load_dwordx4 %0, %1, %2 sc1" : "=v"(st) : "v"(sto), "s"(P.ws + WS_STAT) : "memory");
  }, [&](int k) { b2_early(k); });
  t.stepA(acc, F0, F1, SLOT, [&](int k) { b2_early(3 + k); });
  t.stepB(acc, F1, F0, [&]() { TS(24); wait_vm<7>(); TS(25); bar(); TS(26); }, [&](int k) { b2_early(6 + k); });
  t.stepA(acc, F0, F1, 0, [&](int k) { b2_early(9 + k); });
  TS(27);
  asm volatile("s_waitcnt vmcnt(12)" : "+v"(st) :: "memory");
  TS(28);
  t.mma(acc, F1, [&](int n) {
    if (n == 2) {
      const unsigned tg = (unsigned)tag;
      if (!__all(st.y == tg && st.w == tg)) {
        const GAS u64* sp = (const GAS u64*)(P.ws + WS_STAT + sto);
#pragma unroll 1
        for (unsigned spin = 0; spin < SPIN_CAP; spin++) {
          u64 a = __hip_atomic_load(sp, __ATOMIC_RELAXED, __HIP_MEMORY_SCOPE_AGENT), b = __hip_atomic_load(sp + 1, __ATOMIC_RELAXED, __HIP_MEMORY_SCOPE_AGENT);
          st = (u32x4){(unsigned)a, (unsigned)(a >> 32), (unsigned)b, (unsigned)(b >> 32)};
          if (__all(st.y == tg && st.w == tg)) break;
          __builtin_amdgcn_s_sleep(1);
        }
      }
      *(LAS unsigned*)(smem + ST_MEAN + sti * 4) = st.x; *(LAS unsigned*)(smem + ST_RSTD + sti * 4) = st.z;
    }
  });
  STAMP(2);
  if (H0) {
    if (m == 0) {
      *(LAS f32x4*)(smem + ST_CS + (0 * 64 + c * 16 + 4 * g) * 4) = acc[0][0];
      *(LAS f32x4*)(smem + ST_CS + (1 * 64 + c * 16 + 4 * g) * 4) = acc[0][1];
      if (!QKV) *(LAS f32x4*)(smem + ST_CS + (2 * 64 + c * 16 + 4 * g) * 4) = acc[0][2];
    }
    if (QKV) {
      if (g == 0) *(LAS float*)(smem + ST_CS + (2 * 64 + c * 16 + m) * 4) = acc[0][2][0];
      *(LAS f32x4*)(smem + KVX + c * 2048 + lane * 16) = acc[4][1];
      *(LAS f32x4*)(smem + KVX + c * 2048 + 1024 + lane * 16) = acc[4][2];
    }
  } else {
#pragma unroll
    for (int r = 4; r >= 1; r--)
#pragma unroll
      for (int s = 0; s < 3; s++) acc[r][s] = acc[r - 1][s];
  }
  TS(29);
  wait_vm<5>();
  bar();
  TS(31);
  if (!H0 && QKV) {
    acc[0][1] = *(const LAS f32x4*)(smem + KVX + c * 2048 + lane * 16);
    acc[0][2] = *(const LAS f32x4*)(smem + KVX + c * 2048 + 1024 + lane * 16);
  }
}

__global__ __launch_bounds__(512, 2) void fused_block(Params P) {
  __shared__ __attribute__((aligned(16))) char smem_[163840];
  LAS char* smem = (LAS char*)smem_;
  const unsigned ldsb = (unsigned)(size_t)smem;
  u64 stamps[NSTAMP];
  unsigned tsv = 0;
  TS(0);
  STAMP(0);
  const int tid = threadIdx.x, lane = tid & 63, wave = __builtin_amdgcn_readfirstlane(tid >> 6);
  const int hf = wave >> 2, c = wave & 3, g = lane >> 4, m = lane & 15;
  const int b = blockIdx.x, xcd = b & 7, r5 = b >> 3;
  const int p = (xcd & 1) * 8 + (r5 & 7), jj = r5 >> 3, j = (xcd >> 1) * 2 + (jj & 1);
  const bool qkv = jj < 2;
  const int rot1 = ROT1_MODE == 0 ? 0 : ROT1_MODE == 1 ? (p + jj) : ROT1_MODE == 2 ? ((p + jj) & 1) * 4 : ROT1_MODE == 4 ? ((p + jj) & 3) * 2 : (p + 2 * jj);
  const int rt = xcd * 4 + jj, ct = r5 & 7;

  B2Ctx b2c;
  b2c.wo = P.wo + ct * 128; b2c.w2 = P.w2 + ct * 128; b2c.mboth = ldsb + c * 2048; b2c.mhalf = b2c.mboth + hf * 1024;
  b2c.kq0 = c < 3 ? 30 * c : 89; b2c.nq = c < 2 ? 30 : 29;
  b2c.rot = ROT2_MODE == 0 ? 0 : ROT2_MODE == 1 ? ((ct + rt) & 1) * 15 : ROT2_MODE == 3 ? ((ct + rt) % 3) * 10 : ((ct + rt) & 3) * 7;
  b2c.voff = (lane >> 3) * 1024 + (((lane & 7) ^ (((lane >> 4) & 1) << 2)) * 16);
  b2c.voff1 = b2c.voff + 7168; b2c.vhalf = b2c.voff + hf * 8192;

  f32x4 acc[5][3];
  u64 tag;
  BRegs BR;
  const int tb0 = c * 2048 + (8 * (g >> 1) + (m >> 2)) * 128 + (((2 * (((g & 1) + c) & 3) + ((lane & 3) >> 1)) ^ ((m >> 3) << 2)) * 16) + (lane & 1) * 8;
  if (qkv) { if (hf == 0) phase1<true, true>(P, smem, ldsb, p, j, c, lane, rot1, b2c, acc, tag, stamps, tsv); else phase1<false, true>(P, smem, ldsb, p, j, c, lane, rot1, b2c, acc, tag, stamps, tsv); }
  else     { if (hf == 0) phase1<true, false>(P, smem, ldsb, p, j, c, lane, rot1, b2c, acc, tag, stamps, tsv); else phase1<false, false>(P, smem, ldsb, p, j, c, lane, rot1, b2c, acc, tag, stamps, tsv); }
  STAMP(3);
  const __amdgpu_buffer_rsrc_t act = __builtin_amdgcn_make_buffer_rsrc(P.ws + WS_ACT, 0, ACT_BYTES, 0x00020000);
  if (hf == 1) {
#pragma unroll
    for (int i = 0; i < 28; i++) { br_read(BR, smem, tb0, i); if (i % 4 == 0 && i < 24) b2_issue2(b2c, 12 + i / 4); }
    wait_lgkm0();
    if (lane == 0) *(LAS unsigned*)(smem + PAIRF + c * 4) = 1u;
    TS(43);
  }
  const int lrow0 = hf * 64;
  const int rt32 = p * 4 + hf * 2;
  if (qkv) {
    const f32x4 csq = *(const LAS f32x4*)(smem + ST_CS + (c * 16 + 4 * g) * 4), csk = *(const LAS f32x4*)(smem + ST_CS + (64 + c * 16 + 4 * g) * 4);
    const float csv = *(const LAS float*)(smem + ST_CS + (128 + c * 16 + m) * 4);
    u32x2 qf[5], kf[5], vf[5];
#pragma unroll
    for (int r = 0; r < 5; r++) {
      const float mu = *(const LAS float*)(smem + ST_MEAN + (lrow0 + 16 * r + m) * 4), rs = *(const LAS float*)(smem + ST_RSTD + (lrow0 + 16 * r + m) * 4);
      const f32x4 mu4 = *(const LAS f32x4*)(smem + ST_MEAN + (lrow0 + 16 * r + 4 * g) * 4), rs4 = *(const LAS f32x4*)(smem + ST_RSTD + (lrow0 + 16 * r + 4 * g) * 4);
      f32x4 k4 = (acc[r][1] - mu * csk) * rs, v4 = (acc[r][2] - mu4 * csv) * rs4;
      kf[r] = (u32x2){pk_bf16(k4[0], k4[1]), pk_bf16(k4[2], k4[3])};
      vf[r] = (u32x2){pk_bf16(v4[0], v4[1]), pk_bf16(v4[2], v4[3])};
      if (r > 0) { f32x4 q4v = (acc[r][0] - mu * csq) * rs; qf[r] = (u32x2){pk_bf16(q4v[0], q4v[1]), pk_bf16(q4v[2], q4v[3])}; }
    }
    SB();
    TS(32);
    float NEG = -3.0e38f; asm volatile("" : "+v"(NEG));
    f32x4 mp, mc, mp1;
#pragma unroll
    for (int e = 0; e < 4; e++) {
      const int k = 4 * g + e; const bool par = ((k ^ m) & 1) == 0;
      mp[e] = (par && k >= m + 2) ? 0.f : NEG; mc[e] = (par && k <= m) ? 0.f : NEG;
      mp1[e] = (hf == 0 && p == 0) ? NEG : mp[e];
    }
    const float SC = 0.25f * 1.4426950408889634f;
    float sv[4][4], mx[4];
#pragma unroll
    for (int r = 1; r < 5; r++) {
      f32x4 sp = __builtin_amdgcn_mfma_f32_16x16x16bf16_1k(__builtin_bit_cast(s16x4, kf[r - 1]), __builtin_bit_cast(s16x4, qf[r]), r == 1 ? mp1 : mp, 0, 0, 0);
      f32x4 sc = __builtin_amdgcn_mfma_f32_16x16x16bf16_1k(__builtin_bit_cast(s16x4, kf[r]), __builtin_bit_cast(s16x4, qf[r]), mc, 0, 0, 0);
      sv[r - 1][0] = max3f(sp[0], sp[1], NEG); sv[r - 1][1] = max3f(sp[2], sp[3], NEG); sv[r - 1][2] = max3f(sc[0], sc[1], NEG); sv[r - 1][3] = max3f(sc[2], sc[3], NEG);
      mx[r - 1] = max3f(max3f(sv[r - 1][0], sv[r - 1][1], sv[r - 1][2]), sv[r - 1][3], NEG);
    }
    SB();
    TS(36);
    {
      auto a = __builtin_amdgcn_permlane16_swap(__float_as_uint(mx[0]), __float_as_uint(mx[1]), false, false);
      auto b = __builtin_amdgcn_permlane16_swap(__float_as_uint(mx[2]), __float_as_uint(mx[3]), false, false);
      const float u = max3f(__uint_as_float(a[0]), __uint_as_float(a[1]), NEG), v = max3f(__uint_as_float(b[0]), __uint_as_float(b[1]), NEG);
      auto d = __builtin_amdgcn_permlane32_swap(__float_as_uint(u), __float_as_uint(v), false, false);
      const unsigned t = __float_as_uint(max3f(__uint_as_float(d[0]), __uint_as_float(d[1]), NEG) * SC);
      auto e = __builtin_amdgcn_permlane32_swap(t, t, false, false);
      auto f0 = __builtin_amdgcn_permlane16_swap(e[0], e[0], false, false);
      auto f1 = __builtin_amdgcn_permlane16_swap(e[1], e[1], false, false);
      mx[0] = __uint_as_float(f0[0]); mx[1] = __uint_as_float(f0[1]); mx[2] = __uint_as_float(f1[0]); mx[3] = __uint_as_float(f1[1]);
    }
    const unsigned selA = (m & 1) ? 0x01000c0cu : 0x0c0c0100u, selB = (m & 1) ? 0x03020c0cu : 0x0c0c0302u;
    const u32x4 one4 = {0x3f803f80u, 0x3f803f80u, 0x3f803f80u, 0x3f803f80u};
    const f32x4 z = {0.f, 0.f, 0.f, 0.f};
    f32x4 o4[4], l4[4];
#pragma unroll
    for (int r = 0; r < 4; r++) {
      float e[4];
#pragma unroll
      for (int i = 0; i < 4; i++) e[i] = __builtin_amdgcn_exp2f(sv[r][i] * SC - mx[r]);
      const unsigned pa = pk_bf16(e[0], e[1]), pb = pk_bf16(e[2], e[3]);
      const u32x4 pf = {__builtin_amdgcn_perm(0u, pa, selA), __builtin_amdgcn_perm(0u, pa, selB), __builtin_amdgcn_perm(0u, pb, selA), __builtin_amdgcn_perm(0u, pb, selB)};
      const u32x4 vv = {vf[r].x, vf[r].y, vf[r + 1].x, vf[r + 1].y};
      o4[r] = __builtin_amdgcn_mfma_f32_16x16x32_bf16(b8(vv), b8(pf), z, 0, 0, 0);
      l4[r] = __builtin_amdgcn_mfma_f32_16x16x32_bf16(b8(one4), b8(pf), z, 0, 0, 0);
    }
    SB(); TS(37);
    u32x2 of[4];
#pragma unroll
    for (int r = 0; r < 4; r++) {
      const f32x4 o = o4[r] * __builtin_amdgcn_rcpf(l4[r][0]);
      of[r] = (u32x2){pk_bf16(o[0], o[1]), pk_bf16(o[2], o[3])};
    }
#pragma unroll
    for (int pr = 0; pr < 2; pr++) {
      auto s0 = __builtin_amdgcn_permlane16_swap(of[2 * pr].x, of[1 + 2 * pr].x, false, false);
      auto s1 = __builtin_amdgcn_permlane16_swap(of[2 * pr].y, of[1 + 2 * pr].y, false, false);
      __builtin_amdgcn_raw_buffer_store_b128((u32x4){s0[0], s1[0], s0[1], s1[1]}, act, lane * 16 + ((rt32 + pr) * NK16 + 4 * j + c) * 1024, 0, 16);
    }
    SB();
    STAMP(4);
    TS(38);
  } else {
    const int nblk = (j == 7) ? 9 : 11;
    float mu[4], rs[4]; f32x4 cs[3]; u32x2 b1v[3];
#pragma unroll
    for (int r = 0; r < 4; r++) { mu[r] = *(const LAS float*)(smem + ST_MEAN + (lrow0 + 16 + 16 * r + m) * 4); rs[r] = *(const LAS float*)(smem + ST_RSTD + (lrow0 + 16 + 16 * r + m) * 4); }
#pragma unroll
    for (int s = 0; s < 3; s++) { cs[s] = *(const LAS f32x4*)(smem + ST_CS + (s * 64 + c * 16 + 4 * g) * 4); b1v[s] = *(const LAS u32x2*)(smem + KVX + ((4 * s + c) * 16 + 4 * g) * 2); }
    SB();
    auto gelu_block = [&](int s) {
      const f32x4 bb = {bf_lo(b1v[s].x), bf_hi(b1v[s].x), bf_lo(b1v[s].y), bf_hi(b1v[s].y)};
      u32x2 hfr[4];
#pragma unroll
      for (int r = 0; r < 4; r++) {
        f32x4 v = (acc[r + 1][s] - mu[r] * cs[s]) * rs[r] + bb;
        f32x4 zz = v * (v * v * (-0.10294324f) + (-2.3022082f));
        f32x4 ee = {__builtin_amdgcn_exp2f(zz[0]), __builtin_amdgcn_exp2f(zz[1]), __builtin_amdgcn_exp2f(zz[2]), __builtin_amdgcn_exp2f(zz[3])};
        ee = ee + 1.f;
        f32x4 h = v * (f32x4){__builtin_amdgcn_rcpf(ee[0]), __builtin_amdgcn_rcpf(ee[1]), __builtin_amdgcn_rcpf(ee[2]), __builtin_amdgcn_rcpf(ee[3])};
        hfr[r] = (u32x2){pk_bf16(h[0], h[1]), pk_bf16(h[2], h[3])};
        if (r & 1) {
          const int pr = r >> 1;
          auto s0 = __builtin_amdgcn_permlane16_swap(hfr[2 * pr].x, hfr[2 * pr + 1].x, false, false);
          auto s1 = __builtin_amdgcn_permlane16_swap(hfr[2 * pr].y, hfr[2 * pr + 1].y, false, false);
          __builtin_amdgcn_raw_buffer_store_b128((u32x4){s0[0], s1[0], s0[1], s1[1]}, act, lane * 16 + ((rt32 + pr) * NK16 + 32 + 11 * j + 4 * s + c) * 1024, 0, 16);
        }
      }
      SB();
      TS(32 + s);
    };
    gelu_block(0);
    gelu_block(1);
    if (8 + c < nblk) gelu_block(2);
    STAMP(4);
  }
  TS(40);
  if (hf == 0) {
#pragma unroll
    for (int i = 0; i < 28; i++) br_read(BR, smem, tb0, i);
#pragma unroll 1
    for (unsigned spin = 0; spin < SPIN_CAP; spin++)
      if (__builtin_amdgcn_readfirstlane(__hip_atomic_load((LAS unsigned*)(smem + PAIRF + c * 4), __ATOMIC_RELAXED, __HIP_MEMORY_SCOPE_WORKGROUP)) == 1u) break;
    wait_lgkm0();
#pragma unroll
    for (int s = 18; s < 25; s++) b2_issue2(b2c, s);
    TS(44);
  }
  wait_vm<0>();
  TS(41);
  bar();
  if (tid == 0) __hip_atomic_store((GAS u64*)(P.ws + WS_FLAG + p * 128 + (qkv ? j : 8 + j) * 8), tag, __ATOMIC_RELAXED, __HIP_MEMORY_SCOPE_AGENT);
  STAMP(5);
  TS(42);
#pragma unroll
  for (int i = 28; i < 40; i++) br_read(BR, smem, tb0, i);
  wait_lgkm0();
  TS(46);
  bar();
  TS(47);
#pragma unroll
  for (int s = 25; s < 30; s++) b2_issue(b2c, s);
  const int orow = rt * 64 + hf * 32 + (lane & 31), ocol = ct * 64 + c * 16 + (lane >> 5) * 4;
  const u32x2 xr0 = *(const GAS u32x2*)(P.x + orow * 1024 + ocol * 2), xr1 = *(const GAS u32x2*)(P.x + orow * 1024 + ocol * 2 + 16);
  const u32x2 bv0 = *(const GAS u32x2*)(P.b2 + ocol * 2), bv1 = *(const GAS u32x2*)(P.b2 + ocol * 2 + 16);
  TS(48);
  if (wave == 0) {
    if (b == 0) {
      const GAS u64* fl = (const GAS u64*)(P.ws + WS_FLAG) + lane;
#pragma unroll 1
      for (unsigned spin = 0; spin < SPIN_CAP; spin++) {
        u64 v0 = __hip_atomic_load(fl, __ATOMIC_RELAXED, __HIP_MEMORY_SCOPE_AGENT), v1 = __hip_atomic_load(fl + 64, __ATOMIC_RELAXED, __HIP_MEMORY_SCOPE_AGENT);
        u64 v2 = __hip_atomic_load(fl + 128, __ATOMIC_RELAXED, __HIP_MEMORY_SCOPE_AGENT), v3 = __hip_atomic_load(fl + 192, __ATOMIC_RELAXED, __HIP_MEMORY_SCOPE_AGENT);
        if (__all(v0 == tag && v1 == tag && v2 == tag && v3 == tag)) break;
        __builtin_amdgcn_s_sleep(1);
      }
      if (lane == 0) __hip_atomic_store((GAS u64*)P.ws, tag, __ATOMIC_RELAXED, __HIP_MEMORY_SCOPE_AGENT);
    } else {
      const GAS u64* fl = (const GAS u64*)(P.ws + WS_FLAG + (rt >> 1) * 128 + (lane & 15) * 8);
#pragma unroll 1
      for (unsigned spin = 0; spin < SPIN_CAP; spin++) {
        u64 v = __hip_atomic_load(fl, __ATOMIC_RELAXED, __HIP_MEMORY_SCOPE_AGENT);
        if (__all(v == tag)) break;
        __builtin_amdgcn_s_sleep(1);
      }
    }
  }
  TS(49);
  bar();
  TS(50);
  STAMP(6);
  f32x16 o2[2];
#pragma unroll
  for (int e = 0; e < 16; e++) { o2[0][e] = 0.f; o2[1][e] = 0.f; }
  u32x4 AF[PF];
  const int abase = (rt * 2 + hf) * NK16;
  auto aload = [&](int s) -> u32x4 {
    const int vo = (s == 29 && b2c.nq == 29) ? 0x7f000000 : lane * 16;
    return __builtin_amdgcn_raw_buffer_load_b128(act, vo, (abase + phys_k16(b2c, s)) * 1024, 16);
  };
#pragma unroll
  for (int s = 0; s < PF; s++) AF[s] = aload(s);
#pragma unroll
  for (int s = 0; s < 30; s++) {
    if (s == 25) { wait_lgkm0(); bar(); }
    u32x4 B0, B1;
    if (s < 10) { B0 = (u32x4){BR.lo[s][0].x, BR.lo[s][0].y, BR.hi[s][0].x, BR.hi[s][0].y}; B1 = (u32x4){BR.lo[s][1].x, BR.lo[s][1].y, BR.hi[s][1].x, BR.hi[s][1].y}; }
    else {
      u32x2 lo = trd(smem + b2grp(s) * 8192 + tb0), hi = trd(smem + b2grp(s) * 8192 + tb0 + 512);
      B0 = (u32x4){lo.x, lo.y, hi.x, hi.y};
      lo = trd(smem + b2grp(s) * 8192 + (tb0 ^ 64)); hi = trd(smem + b2grp(s) * 8192 + (tb0 ^ 64) + 512);
      B1 = (u32x4){lo.x, lo.y, hi.x, hi.y};
    }
    const u32x4 a = AF[s % PF];
    o2[0] = __builtin_amdgcn_mfma_f32_32x32x16_bf16(b8(B0), b8(a), o2[0], 0, 0, 0);
    o2[1] = __builtin_amdgcn_mfma_f32_32x32x16_bf16(b8(B1), b8(a), o2[1], 0, 0, 0);
    if (s + PF < 30) AF[s % PF] = aload(s + PF);
    SB();
    if (s == 0) TS(51);
    if (s == 4) TS(52);
    if (s == 9) TS(53);
    if (s == 14) TS(54);
    if (s == 19) TS(55);
    if (s == 24) TS(56);
  }
  STAMP(7);
  TS(57);
  wait_lgkm0();
  bar();
  TS(58);
#pragma unroll
  for (int lb = 1; lb < 4; lb++)
#pragma unroll
    for (int hh = 0; hh < 2; hh++) {
      const int r0 = (lb & 1) * 8 + hh * 4;
      *(LAS f32x4*)(smem + ((hf * 4 + ((c + lb) & 3)) * 3 + lb - 1) * 2048 + hh * 1024 + lane * 16) =
          (f32x4){o2[lb >> 1][r0], o2[lb >> 1][r0 + 1], o2[lb >> 1][r0 + 2], o2[lb >> 1][r0 + 3]};
    }
  f32x4 u0 = {o2[0][0] + (bf_lo(xr0.x) + bf_lo(bv0.x)), o2[0][1] + (bf_hi(xr0.x) + bf_hi(bv0.x)), o2[0][2] + (bf_lo(xr0.y) + bf_lo(bv0.y)), o2[0][3] + (bf_hi(xr0.y) + bf_hi(bv0.y))};
  f32x4 u1 = {o2[0][4] + (bf_lo(xr1.x) + bf_lo(bv1.x)), o2[0][5] + (bf_hi(xr1.x) + bf_hi(bv1.x)), o2[0][6] + (bf_lo(xr1.y) + bf_lo(bv1.y)), o2[0][7] + (bf_hi(xr1.y) + bf_hi(bv1.y))};
  wait_lgkm0();
  TS(59);
  bar();
  TS(60);
#pragma unroll
  for (int i = 0; i < 3; i++) { u0 += *(const LAS f32x4*)(smem + (wave * 3 + i) * 2048 + lane * 16); u1 += *(const LAS f32x4*)(smem + (wave * 3 + i) * 2048 + 1024 + lane * 16); }
  {
    auto s0 = __builtin_amdgcn_permlane32_swap(pk_bf16(u0[0], u0[1]), pk_bf16(u1[0], u1[1]), false, false);
    auto s1 = __builtin_amdgcn_permlane32_swap(pk_bf16(u0[2], u0[3]), pk_bf16(u1[2], u1[3]), false, false);
    *(GAS u32x4*)(P.out + orow * 1024 + (ct * 64 + c * 16 + (lane >> 5) * 8) * 2) = (u32x4){s0[0], s1[0], s0[1], s1[1]};
  }
  STAMP(8);
  TS(61);
  if (DIAG && DIAG != 3) { if (tid == 0) for (int i = 0; i < NSTAMP; i++) ((GAS u64*)(P.ws + WS_DIAG))[blockIdx.x * 16 + i] = stamps[i]; }
}

extern "C" void kernel_launch(void* const* d_in, const int* in_sizes, int n_in, void* d_out, int out_size, void* d_ws, size_t ws_size,
                              hipStream_t stream) {
  Params P;
  P.x = (const char*)d_in[0]; P.wqkv = (const char*)d_in[1]; P.wo = (const char*)d_in[2];
  P.w1 = (const char*)d_in[4]; P.b1 = (const char*)d_in[5]; P.w2 = (const char*)d_in[6]; P.b2 = (const char*)d_in[7];
  P.out = (char*)d_out; P.ws = (char*)d_ws;
  fused_block<<<256, 512, 0, stream>>>(P);
}
```

```cpp
#include <hip/hip_runtime.h>
#include <stdint.h>

typedef __attribute__((ext_vector_type(8))) __bf16 bf16x8;
typedef __attribute__((ext_vector_type(2))) __bf16 bf16x2;
typedef __attribute__((ext_vector_type(4))) short s16x4;
typedef __attribute__((ext_vector_type(4))) float f32x4;
typedef __attribute__((ext_vector_type(2))) float f32x2;
typedef __attribute__((ext_vector_type(16))) float f32x16;
typedef __attribute__((ext_vector_type(4))) unsigned u32x4;
typedef __attribute__((ext_vector_type(2))) unsigned u32x2;
#define LAS __attribute__((address_space(3)))
#define GAS __attribute__((address_space(1)))
typedef unsigned long long u64;

#define DIAG 0
#define PF 5
#define NE 4
#define BG 3
#define ROT1_MODE 1
#define XF 1
#define CPF 1

constexpr int NK16 = 118;
constexpr int SLOT = 43008, XB = 18432;
constexpr int ST_MEAN = 129024, ST_CS = ST_MEAN + 1152, PAIRF = ST_MEAN + 1920, KVX = 19 * 8192;
constexpr int WS_FLAG = 1024, WS_DIAG = 32768, WS_STAT = 65536, WS_ACT = 131072, WS_TS = 16 * 1024 * 1024;
constexpr int ACT_BYTES = 64 * NK16 * 1024;
constexpr int NSTAMP = 9;
constexpr int CODE_LINES = 416;
constexpr unsigned SPIN_CAP = 1u << 20;
constexpr unsigned ONES = 0x3f803f80u;

struct Params {
  const char* x; const char* wqkv; const char* wo; const char* w1; const char* b1; const char* w2; const char* b2;
  char* out; char* ws;
};

#define SB() __builtin_amdgcn_sched_barrier(0)
__device__ __forceinline__ void set_m0(unsigned base, int add) { asm volatile("s_add_i32 m0, %0, %1" :: "s"(base), "i"(add) : "memory", "scc"); }
__device__ __forceinline__ void nop1() { asm volatile("s_nop 0"); }
template <int IMM> __device__ __forceinline__ void glds(unsigned voff, const char* sbase) {
  asm volatile("global_load_lds_dwordx4 %0, %1 offset:%2" :: "v"(voff), "s"(sbase), "i"(IMM) : "memory");
}
__device__ __forceinline__ void glds_mask(unsigned voff, const char* sbase, u64 mask) {
  asm volatile("s_mov_b64 exec, %2\n\tglobal_load_lds_dwordx4 %0, %1\n\ts_mov_b64 exec, -1" :: "v"(voff), "s"(sbase), "s"(mask) : "memory");
}
template <int N> __device__ __forceinline__ void wait_vm() { asm volatile("s_waitcnt vmcnt(%0) lgkmcnt(0)" :: "i"(N) : "memory"); }
__device__ __forceinline__ void wait_lgkm0() { asm volatile("s_waitcnt lgkmcnt(0)" ::: "memory"); }
__device__ __forceinline__ void bar() { asm volatile("s_barrier" ::: "memory"); }
__device__ __forceinline__ unsigned pk_bf16(float a, float b) { f32x2 p = {a, b}; return __builtin_bit_cast(unsigned, __builtin_convertvector(p, bf16x2)); }
__device__ __forceinline__ u32x2 trd(const LAS char* p) { return __builtin_bit_cast(u32x2, __builtin_amdgcn_ds_read_tr16_b64_v4i16((LAS s16x4*)p)); }
__device__ __forceinline__ bf16x8 b8(u32x4 v) { return __builtin_bit_cast(bf16x8, v); }
__device__ __forceinline__ float bf_lo(unsigned v) { return __uint_as_float(v << 16); }
__device__ __forceinline__ float bf_hi(unsigned v) { return __uint_as_float(v & 0xffff0000u); }
__device__ __forceinline__ float add_f32(float a, float b) { float r; asm("v_add_f32 %0, %1, %2" : "=v"(r) : "v"(a), "v"(b)); return r; }
__device__ __forceinline__ float sub_f32(float a, float b) { float r; asm("v_sub_f32 %0, %1, %2" : "=v"(r) : "v"(a), "v"(b)); return r; }
__device__ __forceinline__ float sel0(float v, u64 m) { float r; asm volatile("v_cndmask_b32 %0, %1, 0, %2" : "=v"(r) : "v"(v), "s"(m)); return r; }
__device__ __forceinline__ float max3f(float a, float b, float c) { return fmaxf(fmaxf(a, b), c); }
__device__ __forceinline__ u64 mix64(u64 z) {
  z += 0x9E3779B97F4A7C15ull; z = (z ^ (z >> 30)) * 0xBF58476D1CE4E5B9ull; z = (z ^ (z >> 27)) * 0x94D049BB133111EBull; return z ^ (z >> 31);
}
template <int CTRL> __device__ __forceinline__ float dpp_add(float v) {
  return v + __builtin_bit_cast(float, __builtin_amdgcn_update_dpp(0, __builtin_bit_cast(int, v), CTRL, 0xf, 0xf, true));
}
__device__ __forceinline__ float half_sum(float v) {
  v = dpp_add<0xB1>(v); v = dpp_add<0x4E>(v); v = dpp_add<0x141>(v); v = dpp_add<0x140>(v);
  auto a = __builtin_amdgcn_permlane16_swap(__float_as_uint(v), __float_as_uint(v), false, false);
  return __uint_as_float(a[0]) + __uint_as_float(a[1]);
}
#define LDSP(T, a) ((LAS T*)(size_t)(a))

#define STAMP(i)
#define TS(i)

__device__ __forceinline__ constexpr int b2grp(int s) {
  return s < 3 ? 16 + s : s < 7 ? 8 + s : s < 18 ? s - 7 : s < 21 ? s - 2 : s < 25 ? s - 10 : s < 28 ? s - 25 : s == 28 ? 15 : 19;
}
struct B2Ctx { const char* wo; const char* w2; int a0, rota, f0, nf, rotf; unsigned voff, vhalf, mhalf, mboth; int tb0; };
__device__ __forceinline__ void b2_scalars(B2Ctx& b, const char* wo, const char* w2, unsigned ldsb, int hf, int c, int rt, int ct) {
  b.wo = wo + ct * 128; b.w2 = w2 + ct * 128; b.mboth = ldsb + c * 2048; b.mhalf = b.mboth + hf * 1024;
  b.a0 = 8 * c; b.f0 = c < 3 ? 22 * c : 65; b.nf = c < 2 ? 22 : 21;
  b.rota = ((ct + rt) & 1) * 4; b.rotf = ((ct + rt) & 1) * 10;
}
__device__ __forceinline__ int phys_k16(const B2Ctx& c, int s) {
  if (s < 8) return c.a0 + (s ^ c.rota);
  int t = s - 8 + c.rotf; t = t >= c.nf ? t - c.nf : t;
  return 32 + c.f0 + t;
}
__device__ __forceinline__ const char* b2_src(const B2Ctx& c, int s) {
  return s < 8 ? c.wo + phys_k16(c, s) * 16384 : c.w2 + (phys_k16(c, s) - 32) * 16384;
}
__device__ __forceinline__ void b2_issue(const B2Ctx& c, int s) {
  const char* src = b2_src(c, s);
  set_m0(c.mhalf, b2grp(s) * 8192); nop1();
  glds<0>(c.vhalf, src);
}
__device__ __forceinline__ void b2_issue2(const B2Ctx& c, int s, unsigned voff1) {
  const char* src = b2_src(c, s);
  set_m0(c.mboth, b2grp(s) * 8192); nop1();
  glds<0>(c.voff, src); glds<1024>(voff1, src);
}
struct BRegs { u32x2 lo[10][2], hi[10][2]; };
__device__ __forceinline__ void br_read(BRegs& BR, const LAS char* smem, int tb0, int i) {
  const int s = i >> 2, cb = (i >> 1) & 1;
  if (i & 1) BR.hi[s][cb] = trd(smem + b2grp(s) * 8192 + (tb0 ^ (cb * 64)) + 512); else BR.lo[s][cb] = trd(smem + b2grp(s) * 8192 + (tb0 ^ (cb * 64)));
}

struct Ld { unsigned vo[6]; const char* base0; unsigned mbase; };
struct Epi { f32x4 mp, mc, mp1; unsigned selA, selB; float NEG; float mu[5], rs[5], csv; f32x4 mu4[5], rs4[5], cs[3]; u32x2 b1v[3]; };

template <bool H0, bool QKV, int NS>
struct P1 {
  static constexpr int NRB = H0 ? 5 : 4;
  static constexpr int NL = H0 ? (QKV ? 5 : 4) : 6;
  static constexpr int KSTRIDE = H0 ? 128 : QKV ? 64 * 3072 : 64 * 2752;
  struct Frag { u32x4 A[NRB]; u32x2 Bl[3], Bh[3]; };
  Ld ld; int rot, hdelta; unsigned mbaseh; u64 hmask;
  const char* cb;
  unsigned pb, pa0, pa1;
  u32x4 ones;

  __device__ __forceinline__ void open(int ch) { cb = ld.base0 + ((ch + rot) & 7) * KSTRIDE; set_m0(ld.mbase, (ch % 3) * SLOT); }
  template <int I> __device__ __forceinline__ void ld1(int ch) const {
    if (I >= NL) return;
    if (H0 && I == 4) { set_m0(mbaseh, (ch % 3) * SLOT); glds_mask(ld.vo[4], cb + hdelta, hmask); return; }
    glds<(H0 ? I * 1024 : I * 1024 - 3072)>(ld.vo[I], cb);
  }
  __device__ __forceinline__ void load1(int ch, int i) {
    if (i == 0) { open(ch); nop1(); ld1<0>(ch); } else if (i == 1) ld1<1>(ch); else if (i == 2) ld1<2>(ch);
    else if (i == 3) ld1<3>(ch); else if (i == 4) ld1<4>(ch); else ld1<5>(ch);
  }
  __device__ __forceinline__ void rd(Frag& f, int ks, int n) const {
    constexpr int ord[11] = {0, 1, 6, 2, 3, 4, 5, 7, 8, 9, 10};
    if (n >= 11) return;
    const int i = ord[n];
    if (i < 6) {
      const int s = i >> 1;
      if (s >= NS) return;
      if (i & 1) f.Bh[s] = trd(LDSP(const char, pb) + (32 * ks + 4) * 384 + s * 128); else f.Bl[s] = trd(LDSP(const char, pb) + (32 * ks) * 384 + s * 128);
    } else if (i - 6 < NRB) {
      const int r = H0 ? (i - 5) % 5 : i - 6;
      if (!(H0 && !QKV && r == 0)) f.A[r] = *(const LAS u32x4*)(LDSP(const char, ks ? pa1 : pa0) + r * 2048);
    }
  }
  __device__ __forceinline__ void advance(int delta) {
    asm volatile("v_add_u32 %0, %3, %0\n\tv_add_u32 %1, %3, %1\n\tv_add_u32 %2, %3, %2" : "+v"(pb), "+v"(pa0), "+v"(pa1) : "i"(delta));
  }
  template <class F> __device__ __forceinline__ void mma(f32x4 (&acc)[5][3], Frag& f, F&& fill) const {
#pragma unroll
    for (int rr = 0; rr < NRB; rr++) {
      const int r = H0 ? (rr + 1) % 5 : rr;
#pragma unroll
      for (int s = 0; s < 3; s++) {
        if (s < NS) {
          const u32x4 B = {f.Bl[s].x, f.Bl[s].y, f.Bh[s].x, f.Bh[s].y};
          const u32x4 A = (H0 && !QKV && r == 0) ? ones : f.A[r];
          if (QKV && s == 2) acc[r][s] = __builtin_amdgcn_mfma_f32_16x16x32_bf16(b8(A), b8(B), acc[r][s], 0, 0, 0);
          else acc[r][s] = __builtin_amdgcn_mfma_f32_16x16x32_bf16(b8(B), b8(A), acc[r][s], 0, 0, 0);
          SB();
        }
        fill(3 * rr + s);
        SB();
      }
    }
  }
  template <class F> __device__ __forceinline__ void stepA(f32x4 (&acc)[5][3], Frag& cur, Frag& nx, int delta, F&& site) {
    mma(acc, cur, [&](int n) {
      rd(nx, 1, n);
      constexpr int S0 = H0 ? 4 : 2;
      if (n >= S0 && (n - S0) % 4 == 0 && (n - S0) / 4 < 3) site((n - S0) / 4);
      if (n == 3 * NRB - 2 && delta != 0) advance(delta);
    });
  }
  template <class S, class F> __device__ __forceinline__ void stepB(f32x4 (&acc)[5][3], Frag& cur, Frag& nx, S&& sync, F&& site) const {
    mma(acc, cur, [&](int n) {
      if (n == BG - 1) sync();
      if (n >= BG - 1) { const int i = n - (BG - 1); if (i < 2) { rd(nx, 0, 2 * i); rd(nx, 0, 2 * i + 1); } else rd(nx, 0, i + 2); }
      constexpr int S0 = H0 ? 6 : 5, SD = H0 ? 4 : 3;
      if (n >= S0 && (n - S0) % SD == 0 && (n - S0) / SD < 3) site((n - S0) / SD);
    });
  }
};

template <bool H0, bool QKV, int NSB>
__device__ __forceinline__ void phase1(const Params& P, LAS char* smem, unsigned ldsb, const Ld& ld, int p, int j, int c, int rot, int rt, int ct,
                                       B2Ctx& b2c, f32x4 (&acc)[5][3], Epi& ep, u64& tag, u64 (&stamps)[NSTAMP], unsigned& tsv) {
  typedef P1<H0, QKV, NSB> T;
  constexpr int NL = T::NL, HF = H0 ? 0 : 1;
  const int lane = threadIdx.x & 63;
  T t;
  t.ld = ld; t.rot = rot;
  t.mbaseh = ldsb + 4 * c * 128; t.hdelta = p > 0 ? -16384 : 0; t.hmask = c == 0 ? 0xffff0000ull : 0xffffffffull;
  u64 e_old;
  {
    const int g = lane >> 4, m = lane & 15, q4 = m >> 2, pp = lane & 3;
    t.pa0 = ldsb + (H0 ? 0 : 5 * 2048) + m * 128 + ((g ^ (m >> 1)) * 16); t.pa1 = t.pa0 ^ 64;
    t.pb = ldsb + XB + (8 * g + q4) * 384 + (((2 * c + (pp >> 1)) ^ (2 * ((q4 >> 1) | ((g & 1) << 1)))) * 16) + (pp & 1) * 8;
    const unsigned o = m == 0 ? ONES : 0u; t.ones = (u32x4){o, o, o, o};
    if (H0 && !QKV) asm volatile("" : "+v"(t.ones));
    if (H0 && QKV) { if (c == 0 && lane < 48) *(LAS u32x4*)(smem + g * SLOT + m * 16) = (u32x4){ONES, ONES, ONES, ONES}; }
  }
#pragma unroll
  for (int r = 0; r < 5; r++)
#pragma unroll
    for (int s = 0; s < 3; s++) acc[r][s] = (f32x4){0.f, 0.f, 0.f, 0.f};
  typename T::Frag F0, F1;
  constexpr int NX = H0 ? 2 : 0, NSS = H0 ? 1 : 0;
  const int srow = p * 128 + ((QKV ? 0 : 8) + j) * 8 + 2 * c;
  u32x4 xr0, xr1; float xv[16], mean, sq; unsigned ssa;
  auto row_stats = [&](int k) {
    if (k == 0) {
#pragma unroll
      for (int e = 0; e < 4; e++) { xv[2 * e] = bf_lo(xr0[e]); xv[2 * e + 1] = bf_hi(xr0[e]); xv[8 + 2 * e] = bf_lo(xr1[e]); xv[9 + 2 * e] = bf_hi(xr1[e]); }
      float s0 = xv[0], s1 = xv[8];
#pragma unroll
      for (int e = 1; e < 8; e++) { s0 = add_f32(s0, xv[e]); s1 = add_f32(s1, xv[8 + e]); }
      mean = s0 + s1;
      asm volatile("" : "+v"(mean));
    }
    if (k == 1) { mean = half_sum(mean) * (1.f / 512.f); asm volatile("" : "+v"(mean)); }
    if (k == 2) {
      float q0 = 0.f, q1 = 0.f;
#pragma unroll
      for (int e = 0; e < 8; e++) { float d = sub_f32(xv[e], mean), d2 = sub_f32(xv[8 + e], mean); q0 = fmaf(d, d, q0); q1 = fmaf(d2, d2, q1); asm volatile("" : "+v"(q0), "+v"(q1)); }
      sq = q0 + q1;
      asm volatile("" : "+v"(sq));
    }
    if (k == 3) { sq = __builtin_amdgcn_rsqf(half_sum(sq) * (1.f / 512.f) + 1e-5f); asm volatile("" : "+v"(sq)); }
    if (k == 4) {
      const u32x2 gr = {__float_as_uint((lane & 1) ? sq : mean), (unsigned)tag};
      asm volatile("s_mov_b64 exec, %3\n\tglobal_store_dwordx2 %0, %1, %2 sc1\n\ts_mov_b64 exec, -1" :: "v"(ssa), "v"(gr), "s"(P.ws + WS_STAT), "s"(0x0000000300000003ull) : "memory");
    }
  };
  if (H0) {
    if (!XF) asm volatile("global_load_dwordx4 %0, %2, %3\n\tglobal_load_dwordx4 %1, %2, %3 offset:512" : "=&v"(xr0), "=&v"(xr1) : "v"((lane >> 5) * 1024 + (lane & 31) * 16), "s"(P.x + srow * 1024) : "memory");
    ssa = (srow + (lane >> 5)) * 16 + (lane & 1) * 8; asm volatile("" : "+v"(ssa));
  }
  asm volatile("s_load_dwordx2 %0, %1, 0x0" : "=s"(e_old) : "s"(P.ws) : "memory");
  unsigned pfd = 0, pfo; const char* pfw;
  auto warm = [&](int k) {
    asm volatile("s_mov_b64 exec, %3\n\tglobal_load_dword %0, %1, %2\n\ts_mov_b64 exec, -1" : "+v"(pfd) : "v"(pfo), "s"(pfw), "s"(0x3ull << (2 * k)) : "memory");
  };
  unsigned sto, stw, str1, str4, csw, csw1, kvw, vq[4];
  const char *bA, *bB, *bC;
  auto later = [&](int k) {
    const int g = lane >> 4, m = lane & 15, w = 4 * HF + c;
    if (k == 0) {
      u64 z = mix64(e_old); unsigned lo = __builtin_amdgcn_readfirstlane((unsigned)z), hi = __builtin_amdgcn_readfirstlane((unsigned)(z >> 32));
      asm volatile("" : "+s"(lo), "+s"(hi)); tag = ((u64)hi << 32) | lo;
    }
    if (k == 1) {
      const int l0 = blockIdx.x * 59 + w * 8, lim = blockIdx.x * 59 + 58 - l0;
      const int n0 = 4096 - l0;
      pfw = l0 < 4096 ? P.wo + l0 * 128 : P.w2 + (l0 - 4096) * 128;
      const int top = (l0 < 4096 && n0 - 1 < lim) ? n0 - 1 : lim;
      const int l = lane & 7;
      pfo = (l < top ? l : top) * 128; asm volatile("" : "+v"(pfo), "+s"(pfw));
      const int sti = 18 * w + (lane < 18 ? lane : 17), row = p * 128 - 16 + sti;
      sto = (row < 0 ? 0 : row) * 16; stw = ldsb + ST_MEAN + sti * 4; str1 = ldsb + ST_MEAN + (64 * HF + m) * 4;
      str4 = QKV ? ldsb + ST_MEAN + (64 * HF + 4 * g) * 4 : ldsb + KVX + (c * 16 + 4 * g) * 2;
      csw = ldsb + ST_CS + (c * 16 + 4 * g) * 4; csw1 = ldsb + ST_CS + (128 + c * 16 + m) * 4; kvw = ldsb + KVX + c * 2048 + lane * 16;
      asm volatile("" : "+v"(sto), "+v"(stw), "+v"(str1), "+v"(str4), "+v"(csw));
      if (QKV) asm volatile("" : "+v"(csw1), "+v"(kvw));
      b2c.voff = (lane >> 3) * 1024 + (((lane & 7) ^ ((g & 1) << 2)) * 16);
#pragma unroll
      for (int i = 0; i < 4; i++) vq[i] = b2c.voff + HF * 8192 + i * 16384;
      b2c.tb0 = c * 2048 + (8 * (g >> 1) + (m >> 2)) * 128 + (((2 * (((g & 1) + c) & 3) + ((lane & 3) >> 1)) ^ ((m >> 3) << 2)) * 16) + (lane & 1) * 8;
      asm volatile("" : "+v"(b2c.voff), "+v"(vq[0]), "+v"(vq[1]), "+v"(vq[2]), "+v"(vq[3]), "+v"(b2c.tb0));
      b2c.vhalf = vq[0];
      if (QKV) {
        constexpr u64 MP[4] = {0x555005500050000ull, 0xaaa00aa000a0000ull, 0x1555015500150001ull, 0x2aaa02aa002a0002ull}, MC[4] = {0x5000550055505555ull, 0xa000aa00aaa0aaaaull, 0x4000540055405554ull, 0x8000a800aa80aaa8ull};
        ep.NEG = -3.0e38f; asm volatile("" : "+v"(ep.NEG));
        const u64 live = (H0 && p == 0) ? 0ull : ~0ull;
#pragma unroll
        for (int e = 0; e < 4; e++) {
          ep.mp[e] = sel0(ep.NEG, MP[e]); ep.mc[e] = sel0(ep.NEG, MC[e]);
          ep.mp1[e] = H0 ? sel0(ep.NEG, MP[e] & live) : ep.mp[e];
        }
        ep.selA = (m & 1) ? 0x01000c0cu : 0x0c0c0100u; ep.selB = ep.selA + ((m & 1) ? 0x02020000u : 0x00000202u);
        asm volatile("" : "+v"(ep.selA), "+v"(ep.selB));
      }
    }
    if (k == 2) {
      b2_scalars(b2c, P.wo, P.w2, ldsb, HF, c, rt, ct);
      bA = b2_src(b2c, 0); asm volatile("" : "+s"(bA), "+s"(b2c.mhalf));
    }
    if (k == 3) { bB = b2_src(b2c, 4); asm volatile("" : "+s"(bB)); }
    if (k == 4) { bC = b2_src(b2c, 8); asm volatile("" : "+s"(bC)); }
    if (k == 5 && H0) { if (lane == 0) *LDSP(unsigned, ldsb + PAIRF + c * 4) = 0u; }
  };
  auto b2_early = [&](int i) { set_m0(b2c.mhalf, b2grp(i) * 8192); nop1(); glds<0>(vq[i & 3], i < 4 ? bA : i < 8 ? bB : bC); };
  later(1);
  SB();
  TS(63);
  if (!H0) {
#pragma unroll
    for (int i = 0; i < 6; i++) t.load1(1, i);
  }
  asm volatile("s_waitcnt lgkmcnt(0)" : "+s"(e_old) :: "memory");
  later(0);
  if (H0 && XF) {
    wait_vm<2 * NL>();
    TS(2);
    { const LAS char* q = smem + 16 * 8192 + c * 2048 + (lane >> 5) * 1024 + (lane & 31) * 16; xr0 = *(const LAS u32x4*)q; xr1 = *(const LAS u32x4*)(q + 512); }
#pragma unroll
    for (int k = 0; k < 5; k++) row_stats(k);
  }
  SB();
  TS(3);
  wait_vm<NL + (XF ? NSS : NX)>();
  TS(4);
  bar();
  TS(5);
  STAMP(1);
#pragma unroll
  for (int n = 0; n < 11; n++) t.rd(F0, 0, n);
  t.stepA(acc, F0, F1, SLOT, [&](int k) { t.load1(2, 2 * k); t.load1(2, 2 * k + 1); });
  t.stepB(acc, F1, F0, [&]() { TS(6); wait_vm<NL + (XF ? NSS : NX)>(); TS(7); bar(); TS(8); }, [&](int k) { t.load1(3, k); });
#pragma unroll
  for (int ch = 1; ch < 5; ch++) {
    t.stepA(acc, F0, F1, ch % 3 == 2 ? -2 * SLOT : SLOT, [&](int k) {
      if (H0 && !XF && ch == 2 && k == 0) row_stats(3);
      t.load1(ch + 2, 3 + k);
      if (k == 2) { warm(ch - 1); if (H0 && !XF && ch == 2) row_stats(4); }
    });
    t.stepB(acc, F1, F0, [&]() {
      TS(6 + 3 * ch);
      if (ch == 1) { if (H0 && !XF) asm volatile("s_waitcnt vmcnt(%2) lgkmcnt(0)" : "+v"(xr0), "+v"(xr1) : "i"(NL + 1) : "memory"); else wait_vm<NL + 1>(); }
      else if (ch < 4) wait_vm<NL + 2 + (XF ? 0 : NSS)>();
      else wait_vm<NL + 2>();
      TS(7 + 3 * ch); bar(); TS(8 + 3 * ch);
    }, [&](int k) {
      t.load1(ch + 3, k);
      if (H0 && !XF && ch == 1) row_stats(k);
      if (k == 1) later(1 + ch);
    });
  }
  u32x4 st;
  t.stepA(acc, F0, F1, -2 * SLOT, [&](int k) { t.load1(7, 3 + k); });
  t.stepB(acc, F1, F0, [&]() {
    TS(21); asm volatile("s_waitcnt vmcnt(%1) lgkmcnt(0)" : "+v"(pfd) : "i"(NL) : "memory"); TS(22); bar(); TS(23);
    asm volatile("global_load_dwordx4 %0, %1, %2 sc1" : "=v"(st) : "v"(sto), "s"(P.ws + WS_STAT) : "memory");
  }, [&](int k) { b2_early(k); });
  t.stepA(acc, F0, F1, SLOT, [&](int k) { b2_early(3 + k); });
  t.stepB(acc, F1, F0, [&]() { TS(24); wait_vm<7>(); TS(25); bar(); TS(26); }, [&](int k) { b2_early(6 + k); });
  t.stepA(acc, F0, F1, 0, [&](int k) { b2_early(9 + k); });
  TS(27);
  asm volatile("s_waitcnt vmcnt(12)" : "+v"(st) :: "memory");
  t.mma(acc, F1, [&](int n) {
    if (n == 1) {
      const unsigned tg = (unsigned)tag;
      if (!__all(st.y == tg && st.w == tg)) {
        const GAS u64* sp = (const GAS u64*)(P.ws + WS_STAT + sto);
#pragma unroll 1
        for (unsigned spin = 0; spin < SPIN_CAP; spin++) {
          u64 a = __hip_atomic_load(sp, __ATOMIC_RELAXED, __HIP_MEMORY_SCOPE_AGENT), b = __hip_atomic_load(sp + 1, __ATOMIC_RELAXED, __HIP_MEMORY_SCOPE_AGENT);
          st = (u32x4){(unsigned)a, (unsigned)(a >> 32), (unsigned)b, (unsigned)(b >> 32)};
          if (__all(st.y == tg && st.w == tg)) break;
          __builtin_amdgcn_s_sleep(1);
        }
      }
      *LDSP(unsigned, stw) = st.x; *LDSP(unsigned, stw + 576) = st.z;
    }
  });
  STAMP(2);
  if (H0) {
    if (QKV) {
      *LDSP(f32x4, kvw) = acc[4][1];
      *LDSP(f32x4, kvw + 1024) = acc[4][2];
      if (lane < 16) *LDSP(float, csw1) = acc[0][2][0];
    }
    if ((lane & 15) == 0) {
      *LDSP(f32x4, csw) = acc[0][0];
      *LDSP(f32x4, csw + 256) = acc[0][1];
      if (!QKV) *LDSP(f32x4, csw + 512) = acc[0][2];
    }
  } else {
#pragma unroll
    for (int r = 4; r >= 1; r--)
#pragma unroll
      for (int s = 0; s < 3; s++) acc[r][s] = acc[r - 1][s];
  }
  TS(29);
  wait_vm<12 - NE>();
  if (H0) __builtin_amdgcn_s_setprio(2); else __builtin_amdgcn_s_setprio(1);
  bar();
  TS(31);
  ep.cs[0] = *LDSP(const f32x4, csw); ep.cs[1] = *LDSP(const f32x4, csw + 256);
  if (QKV) ep.csv = *LDSP(const float, csw1); else ep.cs[2] = *LDSP(const f32x4, csw + 512);
  if (!H0 && QKV) { acc[0][1] = *LDSP(const f32x4, kvw); acc[0][2] = *LDSP(const f32x4, kvw + 1024); }
#pragma unroll
  for (int r = QKV ? 0 : 1; r < 5; r++) {
    ep.mu[r] = *LDSP(const float, str1 + 64 * r); ep.rs[r] = *LDSP(const float, str1 + 64 * r + 576);
    if (QKV) { ep.mu4[r] = *LDSP(const f32x4, str4 + 64 * r); ep.rs4[r] = *LDSP(const f32x4, str4 + 64 * r + 576); }
    else if (r < 4) ep.b1v[r - 1] = *LDSP(const u32x2, str4 + 128 * (r - 1));
  }
  SB();
}

typedef __attribute__((ext_vector_type(16))) unsigned u32x16;
__device__ __forceinline__ char* mkptr(unsigned lo, unsigned hi) { return (char*)(((u64)hi << 32) | lo); }
__global__ __launch_bounds__(512, 2) void fused_block(Params) {
  u64 stamps[NSTAMP];
  unsigned tsv = 0;
  TS(0);
  STAMP(0);
  u32x16 ka; u32x2 kb;
  asm volatile("s_load_dwordx16 %0, %2, 0x0\n\ts_load_dwordx2 %1, %2, 0x40" : "=&s"(ka), "=&s"(kb) : "s"(__builtin_amdgcn_kernarg_segment_ptr()));
  Params P;
  auto args = [&]() {
    asm volatile("s_waitcnt lgkmcnt(0)" : "+s"(ka), "+s"(kb));
    P.x = mkptr(ka[0], ka[1]); P.wqkv = mkptr(ka[2], ka[3]); P.wo = mkptr(ka[4], ka[5]); P.w1 = mkptr(ka[6], ka[7]); P.b1 = mkptr(ka[8], ka[9]);
    P.w2 = mkptr(ka[10], ka[11]); P.b2 = mkptr(ka[12], ka[13]); P.out = mkptr(ka[14], ka[15]); P.ws = mkptr(kb[0], kb[1]);
  };
  __shared__ __attribute__((aligned(16))) char smem_[163840];
  LAS char* smem = (LAS char*)smem_;
  const unsigned ldsb = (unsigned)(size_t)smem;
  const int tid = threadIdx.x, lane = tid & 63, wave = __builtin_amdgcn_readfirstlane(tid >> 6);
  const int b = blockIdx.x, xcd = b & 7, r5 = b >> 3;
  unsigned cpd = 0;
  if (CPF == 1) {
    u64 pc; asm volatile("s_getpc_b64 %0" : "=s"(pc)); pc &= ~255ull;
    asm volatile("global_load_dword %0, %1, %2" : "+v"(cpd) : "v"(((tid < CODE_LINES / 32 ? tid : 0) * 32 + r5) * 128), "s"(pc) : "memory");
  }
  const int hf = wave >> 2, c = wave & 3, jj = r5 >> 3;
  const bool qkv = jj < 2;
  const int p = (xcd & 1) * 8 + (r5 & 7), j = (xcd >> 1) * 2 + (jj & 1);
  const int rot1 = ROT1_MODE == 0 ? 0 : ROT1_MODE == 1 ? (p + jj) : ROT1_MODE == 2 ? ((p + jj) & 1) * 4 : ((p + jj) & 3) * 2;
  const int rt = xcd * 4 + jj, ct = r5 & 7;
  Ld ld;
  const int pitch = qkv ? 3072 : 2752, k0 = rot1 & 7, k1 = (rot1 + 1) & 7;
  int so0, ko0, ko1;
  if (hf == 0) {
    so0 = p * 131072 - 4096; ko0 = k0 * 128; ko1 = k1 * 128; ld.mbase = ldsb + (16 + 32 * c) * 128;
    asm volatile("" : "+s"(so0), "+s"(ko0), "+s"(ko1), "+s"(ld.mbase));
    const unsigned v0 = 4096 + (32 * c + (lane >> 3)) * 1024 + (((lane & 7) ^ (lane >> 4)) * 16);
    ld.vo[0] = v0; ld.vo[1] = (v0 ^ 64) + 7168; ld.vo[2] = v0 + 14336; ld.vo[3] = (v0 ^ 64) + 21504;
    { const int row = 4 * c + ((lane >> 3) & 3); ld.vo[4] = 4096 + row * 1024 + (((lane & 7) ^ (row >> 1)) * 16); }
    ld.vo[5] = (lane < (j == 7 ? 18 : 22) ? lane : 0) * 16;
  } else {
    const int secst = qkv ? 1024 : 128, lim = qkv ? 23 : (j == 7 ? 17 : 21);
    so0 = j * (qkv ? 128 : 352) + c * 16 * pitch - 4096; ko0 = k0 * 64 * pitch; ko1 = k1 * 64 * pitch; ld.mbase = ldsb + XB + c * 6144 + 3072;
    asm volatile("" : "+s"(so0), "+s"(ko0), "+s"(ko1), "+s"(ld.mbase));
#pragma unroll
    for (int r = 0; r < 3; r++) {
      const int v = lane + 64 * r, row = (v * 171) >> 12, cc = (v - 24 * row) ^ (row & 2);
      const int c0 = cc < lim ? cc : lim, c1 = (cc ^ 4) < lim ? (cc ^ 4) : lim;
      ld.vo[r] = 4096 + 3072 - 1024 * r + row * pitch + (c0 >> 3) * secst + (c0 & 7) * 16;
      ld.vo[r + 3] = 4096 - 1024 * r + (row + 8) * pitch + (c1 >> 3) * secst + (c1 & 7) * 16;
    }
  }
  unsigned xvo = lane * 16;
  asm volatile("" : "+v"(ld.vo[0]), "+v"(ld.vo[1]), "+v"(ld.vo[2]), "+v"(ld.vo[3]), "+v"(ld.vo[4]), "+v"(ld.vo[5]), "+v"(xvo));
  SB();
  args();
  ld.base0 = (hf == 0 ? P.x : qkv ? P.wqkv : P.w1) + so0;
  TS(62);
  if (hf == 0) {
    const u64 hmask = c == 0 ? 0xffff0000ull : 0xffffffffull;
    if (XF) {
      const char* xrow = P.x + (p * 128 + ((qkv ? 0 : 8) + j) * 8 + 2 * c) * 1024;
      set_m0(ldsb + c * 2048, 16 * 8192); nop1(); glds<0>(xvo, xrow); glds<1024>(xvo, xrow);
    }
#pragma unroll
    for (int ch = 0; ch < 2; ch++) {
      const char* cb = ld.base0 + (ch ? ko1 : ko0);
      set_m0(ld.mbase, ch * SLOT); nop1();
      glds<0>(ld.vo[0], cb); glds<1024>(ld.vo[1], cb); glds<2048>(ld.vo[2], cb); glds<3072>(ld.vo[3], cb);
      if (qkv) { set_m0(ldsb + 4 * c * 128, ch * SLOT); glds_mask(ld.vo[4], cb + (p > 0 ? -16384 : 0), hmask); }
    }
    if (!qkv && c == 0) { set_m0(ldsb, KVX); nop1(); glds<0>(ld.vo[5], P.b1 + j * 352); }
  } else {
    const char* cb = ld.base0 + ko0;
    set_m0(ld.mbase, 0); nop1();
    glds<-3072>(ld.vo[0], cb); glds<-2048>(ld.vo[1], cb); glds<-1024>(ld.vo[2], cb); glds<0>(ld.vo[3], cb); glds<1024>(ld.vo[4], cb); glds<2048>(ld.vo[5], cb);
  }
  SB();
  TS(1);
  B2Ctx b2c; f32x4 acc[5][3]; u64 tag; Epi ep;
#define P1ARGS P, smem, ldsb, ld, p, j, c, rot1, rt, ct, b2c, acc, ep, tag, stamps, tsv
  const bool blk2 = 8 + c < ((j == 7) ? 9 : 11);
  if (qkv) { if (hf == 0) phase1<true, true, 3>(P1ARGS); else phase1<false, true, 3>(P1ARGS); }
  else if (blk2) { if (hf == 0) phase1<true, false, 3>(P1ARGS); else phase1<false, false, 3>(P1ARGS); }
  else { if (hf == 0) phase1<true, false, 2>(P1ARGS); else phase1<false, false, 2>(P1ARGS); }
  STAMP(3);
  asm volatile("" :: "v"(cpd));
  b2_scalars(b2c, P.wo, P.w2, ldsb, hf, c, rt, ct);
  const __amdgpu_buffer_rsrc_t act = __builtin_amdgcn_make_buffer_rsrc(P.ws + WS_ACT, 0, ACT_BYTES, 0x00020000);
  const int rt32 = p * 4 + hf * 2, tb0 = b2c.tb0;
  BRegs BR;
  auto side = [&](int u) {
    SB();
    b2_issue(b2c, 12 + u);
    if (hf == 1) {
      if (u < 4) {
#pragma unroll
        for (int i = 0; i < NE; i++) br_read(BR, smem, tb0, NE * u + i);
      }
      if (u == 5) { wait_lgkm0(); if (lane == 0) *(LAS unsigned*)(smem + PAIRF + c * 4) = 1u; }
    }
    SB();
  };
  if (qkv) {
    u32x2 qf[5], kf[5], vf[5];
#pragma unroll
    for (int r = 0; r < 5; r++) {
      f32x4 k4 = (acc[r][1] - ep.mu[r] * ep.cs[1]) * ep.rs[r], v4 = (acc[r][2] - ep.mu4[r] * ep.csv) * ep.rs4[r];
      kf[r] = (u32x2){pk_bf16(k4[0], k4[1]), pk_bf16(k4[2], k4[3])};
      vf[r] = (u32x2){pk_bf16(v4[0], v4[1]), pk_bf16(v4[2], v4[3])};
      if (r > 0) { f32x4 q4v = (acc[r][0] - ep.mu[r] * ep.cs[0]) * ep.rs[r]; qf[r] = (u32x2){pk_bf16(q4v[0], q4v[1]), pk_bf16(q4v[2], q4v[3])}; }
      side(r);
    }
    TS(32);
    const float SC = 0.25f * 1.4426950408889634f, NEG = ep.NEG;
    float sv[4][4], mx[4];
#pragma unroll
    for (int r = 1; r < 5; r++) {
      f32x4 sp = __builtin_amdgcn_mfma_f32_16x16x16bf16_1k(__builtin_bit_cast(s16x4, kf[r - 1]), __builtin_bit_cast(s16x4, qf[r]), r == 1 ? ep.mp1 : ep.mp, 0, 0, 0);
      f32x4 sc = __builtin_amdgcn_mfma_f32_16x16x16bf16_1k(__builtin_bit_cast(s16x4, kf[r]), __builtin_bit_cast(s16x4, qf[r]), ep.mc, 0, 0, 0);
      sv[r - 1][0] = max3f(sp[0], sp[1], NEG); sv[r - 1][1] = max3f(sp[2], sp[3], NEG); sv[r - 1][2] = max3f(sc[0], sc[1], NEG); sv[r - 1][3] = max3f(sc[2], sc[3], NEG);
      mx[r - 1] = max3f(max3f(sv[r - 1][0], sv[r - 1][1], sv[r - 1][2]), sv[r - 1][3], NEG);
    }
    side(5);
    TS(36);
    {
      auto a = __builtin_amdgcn_permlane16_swap(__float_as_uint(mx[0]), __float_as_uint(mx[1]), false, false);
      auto b = __builtin_amdgcn_permlane16_swap(__float_as_uint(mx[2]), __float_as_uint(mx[3]), false, false);
      const float u = max3f(__uint_as_float(a[0]), __uint_as_float(a[1]), NEG), v = max3f(__uint_as_float(b[0]), __uint_as_float(b[1]), NEG);
      auto d = __builtin_amdgcn_permlane32_swap(__float_as_uint(u), __float_as_uint(v), false, false);
      const unsigned t = __float_as_uint(max3f(__uint_as_float(d[0]), __uint_as_float(d[1]), NEG) * SC);
      auto e = __builtin_amdgcn_permlane32_swap(t, t, false, false);
      auto f0 = __builtin_amdgcn_permlane16_swap(e[0], e[0], false, false);
      auto f1 = __builtin_amdgcn_permlane16_swap(e[1], e[1], false, false);
      mx[0] = __uint_as_float(f0[0]); mx[1] = __uint_as_float(f0[1]); mx[2] = __uint_as_float(f1[0]); mx[3] = __uint_as_float(f1[1]);
    }
    const u32x4 one4 = {ONES, ONES, ONES, ONES};
    const f32x4 z = {0.f, 0.f, 0.f, 0.f};
    f32x4 o4[4], l4[4];
#pragma unroll
    for (int r = 0; r < 4; r++) {
      float e[4];
#pragma unroll
      for (int i = 0; i < 4; i++) e[i] = __builtin_amdgcn_exp2f(sv[r][i] * SC - mx[r]);
      const unsigned pa = pk_bf16(e[0], e[1]), pb = pk_bf16(e[2], e[3]);
      const u32x4 pf = {__builtin_amdgcn_perm(0u, pa, ep.selA), __builtin_amdgcn_perm(0u, pa, ep.selB), __builtin_amdgcn_perm(0u, pb, ep.selA), __builtin_amdgcn_perm(0u, pb, ep.selB)};
      const u32x4 vv = {vf[r].x, vf[r].y, vf[r + 1].x, vf[r + 1].y};
      o4[r] = __builtin_amdgcn_mfma_f32_16x16x32_bf16(b8(vv), b8(pf), z, 0, 0, 0);
      l4[r] = __builtin_amdgcn_mfma_f32_16x16x32_bf16(b8(one4), b8(pf), z, 0, 0, 0);
    }
    u32x2 of[4];
#pragma unroll
    for (int r = 0; r < 4; r++) {
      const f32x4 o = o4[r] * __builtin_amdgcn_rcpf(l4[r][0]);
      of[r] = (u32x2){pk_bf16(o[0], o[1]), pk_bf16(o[2], o[3])};
    }
#pragma unroll
    for (int pr = 0; pr < 2; pr++) {
      auto s0 = __builtin_amdgcn_permlane16_swap(of[2 * pr].x, of[1 + 2 * pr].x, false, false);
      auto s1 = __builtin_amdgcn_permlane16_swap(of[2 * pr].y, of[1 + 2 * pr].y, false, false);
      __builtin_amdgcn_raw_buffer_store_b128((u32x4){s0[0], s1[0], s0[1], s1[1]}, act, lane * 16 + ((rt32 + pr) * NK16 + 4 * j + c) * 1024, 0, 16);
    }
    SB();
  } else {
    auto gelu_block = [&](int s) {
      const f32x4 bb = {bf_lo(ep.b1v[s].x), bf_hi(ep.b1v[s].x), bf_lo(ep.b1v[s].y), bf_hi(ep.b1v[s].y)};
      u32x2 hfr[4];
#pragma unroll
      for (int r = 0; r < 4; r++) {
        if (4 * s + r < 6) side(4 * s + r);
        f32x4 v = (acc[r + 1][s] - ep.mu[r + 1] * ep.cs[s]) * ep.rs[r + 1] + bb;
        f32x4 zz = v * (v * v * (-0.10294324f) + (-2.3022082f));
        f32x4 ee = {__builtin_amdgcn_exp2f(zz[0]), __builtin_amdgcn_exp2f(zz[1]), __builtin_amdgcn_exp2f(zz[2]), __builtin_amdgcn_exp2f(zz[3])};
        ee = ee + 1.f;
        f32x4 h = v * (f32x4){__builtin_amdgcn_rcpf(ee[0]), __builtin_amdgcn_rcpf(ee[1]), __builtin_amdgcn_rcpf(ee[2]), __builtin_amdgcn_rcpf(ee[3])};
        hfr[r] = (u32x2){pk_bf16(h[0], h[1]), pk_bf16(h[2], h[3])};
        if (r & 1) {
          const int pr = r >> 1;
          auto s0 = __builtin_amdgcn_permlane16_swap(hfr[2 * pr].x, hfr[2 * pr + 1].x, false, false);
          auto s1 = __builtin_amdgcn_permlane16_swap(hfr[2 * pr].y, hfr[2 * pr + 1].y, false, false);
          __builtin_amdgcn_raw_buffer_store_b128((u32x4){s0[0], s1[0], s0[1], s1[1]}, act, lane * 16 + ((rt32 + pr) * NK16 + 32 + 11 * j + 4 * s + c) * 1024, 0, 16);
        }
      }
      SB();
      TS(32 + s);
    };
    gelu_block(0);
    gelu_block(1);
    if (blk2) gelu_block(2);
  }
  STAMP(4);
  TS(40);
  if (hf == 0) {
    __builtin_amdgcn_s_setprio(0);
#pragma unroll
    for (int i = 0; i < 4 * NE; i++) br_read(BR, smem, tb0, i);
#pragma unroll 1
    for (unsigned spin = 0; spin < SPIN_CAP; spin++)
      if (__builtin_amdgcn_readfirstlane(__hip_atomic_load((LAS unsigned*)(smem + PAIRF + c * 4), __ATOMIC_RELAXED, __HIP_MEMORY_SCOPE_WORKGROUP)) == 1u) break;
    wait_lgkm0();
#pragma unroll
    for (int s = 18; s < 18 + NE; s++) b2_issue2(b2c, s, b2c.voff + 7168);
    TS(44);
    wait_vm<2 * NE>();
  } else {
    wait_vm<0>();
    __builtin_amdgcn_s_setprio(0);
  }
  TS(41);
  bar();
  if (tid == 0) __hip_atomic_store((GAS u64*)(P.ws + WS_FLAG + p * 128 + (qkv ? j : 8 + j) * 8), tag, __ATOMIC_RELAXED, __HIP_MEMORY_SCOPE_AGENT);
  STAMP(5);
  TS(42);
#pragma unroll
  for (int i = 4 * NE; i < 40; i++) br_read(BR, smem, tb0, i);
  wait_lgkm0();
  bar();
#pragma unroll
  for (int s = 18 + NE; s < 30; s++) b2_issue(b2c, s);
  const int orow = rt * 64 + hf * 32 + (lane & 31), ocol = ct * 64 + c * 16 + (lane >> 5) * 4;
  const u32x2 xs0 = *(const GAS u32x2*)(P.x + orow * 1024 + ocol * 2), xs1 = *(const GAS u32x2*)(P.x + orow * 1024 + ocol * 2 + 16);
  const u32x2 bv0 = *(const GAS u32x2*)(P.b2 + ocol * 2), bv1 = *(const GAS u32x2*)(P.b2 + ocol * 2 + 16);
  TS(45);
  if (wave == 0) {
    if (b == 0) {
      const GAS u64* fa = (const GAS u64*)(P.ws + WS_FLAG) + lane;
#pragma unroll 1
      for (unsigned spin = 0; spin < SPIN_CAP; spin++) {
        u64 v0 = __hip_atomic_load(fa, __ATOMIC_RELAXED, __HIP_MEMORY_SCOPE_AGENT), v1 = __hip_atomic_load(fa + 64, __ATOMIC_RELAXED, __HIP_MEMORY_SCOPE_AGENT);
        u64 v2 = __hip_atomic_load(fa + 128, __ATOMIC_RELAXED, __HIP_MEMORY_SCOPE_AGENT), v3 = __hip_atomic_load(fa + 192, __ATOMIC_RELAXED, __HIP_MEMORY_SCOPE_AGENT);
        if (__all(v0 == tag && v1 == tag && v2 == tag && v3 == tag)) break;
        __builtin_amdgcn_s_sleep(1);
      }
      if (lane == 0) __hip_atomic_store((GAS u64*)P.ws, tag, __ATOMIC_RELAXED, __HIP_MEMORY_SCOPE_AGENT);
    } else {
      const GAS u64* fl = (const GAS u64*)(P.ws + WS_FLAG + (rt >> 1) * 128 + (lane & 15) * 8);
#pragma unroll 1
      for (unsigned spin = 0; spin < SPIN_CAP; spin++) {
        if (__all(__hip_atomic_load(fl, __ATOMIC_RELAXED, __HIP_MEMORY_SCOPE_AGENT) == tag)) break;
        __builtin_amdgcn_s_sleep(1);
      }
    }
  }
  TS(49);
  bar();
  TS(50);
  STAMP(6);
  const int abase = (rt * 2 + hf) * NK16;
  auto aload = [&](int s) -> u32x4 {
    const int vo = (s == 29 && b2c.nf == 21) ? 0x7f000000 : lane * 16;
    return __builtin_amdgcn_raw_buffer_load_b128(act, vo, (abase + phys_k16(b2c, s)) * 1024, 16);
  };
  u32x4 AF[PF];
  f32x16 o2[2];
#pragma unroll
  for (int e = 0; e < 16; e++) { o2[0][e] = 0.f; o2[1][e] = 0.f; }
#pragma unroll
  for (int s = 0; s < PF; s++) AF[s] = aload(s);
#pragma unroll
  for (int s = 0; s < 30; s++) {
    if (s == 18) { wait_lgkm0(); bar(); }
    u32x4 B0, B1;
    if (s < 10) { B0 = (u32x4){BR.lo[s][0].x, BR.lo[s][0].y, BR.hi[s][0].x, BR.hi[s][0].y}; B1 = (u32x4){BR.lo[s][1].x, BR.lo[s][1].y, BR.hi[s][1].x, BR.hi[s][1].y}; }
    else {
      u32x2 lo = trd(smem + b2grp(s) * 8192 + tb0), hi = trd(smem + b2grp(s) * 8192 + tb0 + 512);
      B0 = (u32x4){lo.x, lo.y, hi.x, hi.y};
      lo = trd(smem + b2grp(s) * 8192 + (tb0 ^ 64)); hi = trd(smem + b2grp(s) * 8192 + (tb0 ^ 64) + 512);
      B1 = (u32x4){lo.x, lo.y, hi.x, hi.y};
    }
    const u32x4 a = AF[s % PF];
    o2[0] = __builtin_amdgcn_mfma_f32_32x32x16_bf16(b8(B0), b8(a), o2[0], 0, 0, 0);
    o2[1] = __builtin_amdgcn_mfma_f32_32x32x16_bf16(b8(B1), b8(a), o2[1], 0, 0, 0);
    if (s + PF < 30) AF[s % PF] = aload(s + PF);
    SB();
    if (s == 0) TS(51);
    if (s == 9) TS(53);
    if (s == 19) TS(55);
  }
  STAMP(7);
  TS(57);
  wait_lgkm0();
  bar();
  TS(58);
#pragma unroll
  for (int lb = 1; lb < 4; lb++)
#pragma unroll
    for (int hh = 0; hh < 2; hh++) {
      const int r0 = (lb & 1) * 8 + hh * 4;
      *(LAS f32x4*)(smem + ((hf * 4 + ((c + lb) & 3)) * 3 + lb - 1) * 2048 + hh * 1024 + lane * 16) =
          (f32x4){o2[lb >> 1][r0], o2[lb >> 1][r0 + 1], o2[lb >> 1][r0 + 2], o2[lb >> 1][r0 + 3]};
    }
  f32x4 u0 = {o2[0][0] + (bf_lo(xs0.x) + bf_lo(bv0.x)), o2[0][1] + (bf_hi(xs0.x) + bf_hi(bv0.x)), o2[0][2] + (bf_lo(xs0.y) + bf_lo(bv0.y)), o2[0][3] + (bf_hi(xs0.y) + bf_hi(bv0.y))};
  f32x4 u1 = {o2[0][4] + (bf_lo(xs1.x) + bf_lo(bv1.x)), o2[0][5] + (bf_hi(xs1.x) + bf_hi(bv1.x)), o2[0][6] + (bf_lo(xs1.y) + bf_lo(bv1.y)), o2[0][7] + (bf_hi(xs1.y) + bf_hi(bv1.y))};
  wait_lgkm0();
  TS(59);
  bar();
  TS(60);
#pragma unroll
  for (int i = 0; i < 3; i++) { u0 += *(const LAS f32x4*)(smem + (wave * 3 + i) * 2048 + lane * 16); u1 += *(const LAS f32x4*)(smem + (wave * 3 + i) * 2048 + 1024 + lane * 16); }
  {
    auto s0 = __builtin_amdgcn_permlane32_swap(pk_bf16(u0[0], u0[1]), pk_bf16(u1[0], u1[1]), false, false);
    auto s1 = __builtin_amdgcn_permlane32_swap(pk_bf16(u0[2], u0[3]), pk_bf16(u1[2], u1[3]), false, false);
    *(GAS u32x4*)(P.out + orow * 1024 + (ct * 64 + c * 16 + (lane >> 5) * 8) * 2) = (u32x4){s0[0], s1[0], s0[1], s1[1]};
  }
  STAMP(8);
  TS(61);
}

extern "C" void kernel_launch(void* const* d_in, const int* in_sizes, int n_in, void* d_out, int out_size, void* d_ws, size_t ws_size,
                              hipStream_t stream) {
  Params P;
  P.x = (const char*)d_in[0]; P.wqkv = (const char*)d_in[1]; P.wo = (const char*)d_in[2];
  P.w1 = (const char*)d_in[4]; P.b1 = (const char*)d_in[5]; P.w2 = (const char*)d_in[6]; P.b2 = (const char*)d_in[7];
  P.out = (char*)d_out; P.ws = (char*)d_ws;
  fused_block<<<256, 512, 0, stream>>>(P);
}
```

```cpp
#define DIAG 1
#define AMP 0
#include <hip/hip_runtime.h>
#include <stdint.h>

typedef __attribute__((ext_vector_type(8))) __bf16 bf16x8;
typedef __attribute__((ext_vector_type(2))) __bf16 bf16x2;
typedef __attribute__((ext_vector_type(4))) short s16x4;
typedef __attribute__((ext_vector_type(4))) float f32x4;
typedef __attribute__((ext_vector_type(2))) float f32x2;
typedef __attribute__((ext_vector_type(16))) float f32x16;
typedef __attribute__((ext_vector_type(4))) unsigned u32x4;
typedef __attribute__((ext_vector_type(2))) unsigned u32x2;
#define LAS __attribute__((address_space(3)))
#define GAS __attribute__((address_space(1)))
typedef unsigned long long u64;

#define PF 5
#define NE 4
#define BG 3
#define ROT1_MODE 1
#define XF 1
#define CPF 1

constexpr int NK16 = 118;
constexpr int SLOT = 43008, XB = 18432;
constexpr int ST_MEAN = 129024, ST_CS = ST_MEAN + 1152, PAIRF = ST_MEAN + 1920, KVX = 19 * 8192;
constexpr int WS_FLAG = 1024, WS_DIAG = 32768, WS_STAT = 65536, WS_ACT = 131072, WS_TS = 16 * 1024 * 1024;
constexpr int ACT_BYTES = 64 * NK16 * 1024;
constexpr int NSTAMP = 9;
constexpr int CODE_LINES = 416;
constexpr unsigned SPIN_CAP = 1u << 20;
constexpr unsigned ONES = 0x3f803f80u;

struct Params {
  const char* x; const char* wqkv; const char* wo; const char* w1; const char* b1; const char* w2; const char* b2;
  char* out; char* ws;
};

#define SB() __builtin_amdgcn_sched_barrier(0)
__device__ __forceinline__ void set_m0(unsigned base, int add) { asm volatile("s_add_i32 m0, %0, %1" :: "s"(base), "i"(add) : "memory", "scc"); }
__device__ __forceinline__ void nop1() { asm volatile("s_nop 0"); }
template <int IMM> __device__ __forceinline__ void glds(unsigned voff, const char* sbase) {
  asm volatile("global_load_lds_dwordx4 %0, %1 offset:%2" :: "v"(voff), "s"(sbase), "i"(IMM) : "memory");
}
__device__ __forceinline__ void glds_mask(unsigned voff, const char* sbase, u64 mask) {
  asm volatile("s_mov_b64 exec, %2\n\tglobal_load_lds_dwordx4 %0, %1\n\ts_mov_b64 exec, -1" :: "v"(voff), "s"(sbase), "s"(mask) : "memory");
}
template <int N> __device__ __forceinline__ void wait_vm() { asm volatile("s_waitcnt vmcnt(%0) lgkmcnt(0)" :: "i"(N) : "memory"); }
__device__ __forceinline__ void wait_lgkm0() { asm volatile("s_waitcnt lgkmcnt(0)" ::: "memory"); }
__device__ __forceinline__ void bar() { asm volatile("s_barrier" ::: "memory"); }
__device__ __forceinline__ unsigned pk_bf16(float a, float b) { f32x2 p = {a, b}; return __builtin_bit_cast(unsigned, __builtin_convertvector(p, bf16x2)); }
__device__ __forceinline__ u32x2 trd(const LAS char* p) { return __builtin_bit_cast(u32x2, __builtin_amdgcn_ds_read_tr16_b64_v4i16((LAS s16x4*)p)); }
__device__ __forceinline__ bf16x8 b8(u32x4 v) { return __builtin_bit_cast(bf16x8, v); }
__device__ __forceinline__ float bf_lo(unsigned v) { return __uint_as_float(v << 16); }
__device__ __forceinline__ float bf_hi(unsigned v) { return __uint_as_float(v & 0xffff0000u); }
__device__ __forceinline__ float add_f32(float a, float b) { float r; asm("v_add_f32 %0, %1, %2" : "=v"(r) : "v"(a), "v"(b)); return r; }
__device__ __forceinline__ float sub_f32(float a, float b) { float r; asm("v_sub_f32 %0, %1, %2" : "=v"(r) : "v"(a), "v"(b)); return r; }
__device__ __forceinline__ float sel0(float v, u64 m) { float r; asm volatile("v_cndmask_b32 %0, %1, 0, %2" : "=v"(r) : "v"(v), "s"(m)); return r; }
__device__ __forceinline__ float max3f(float a, float b, float c) { return fmaxf(fmaxf(a, b), c); }
__device__ __forceinline__ u64 mix64(u64 z) {
  z += 0x9E3779B97F4A7C15ull; z = (z ^ (z >> 30)) * 0xBF58476D1CE4E5B9ull; z = (z ^ (z >> 27)) * 0x94D049BB133111EBull; return z ^ (z >> 31);
}
template <int CTRL> __device__ __forceinline__ float dpp_add(float v) {
  return v + __builtin_bit_cast(float, __builtin_amdgcn_update_dpp(0, __builtin_bit_cast(int, v), CTRL, 0xf, 0xf, true));
}
__device__ __forceinline__ float half_sum(float v) {
  v = dpp_add<0xB1>(v); v = dpp_add<0x4E>(v); v = dpp_add<0x141>(v); v = dpp_add<0x140>(v);
  auto a = __builtin_amdgcn_permlane16_swap(__float_as_uint(v), __float_as_uint(v), false, false);
  return __uint_as_float(a[0]) + __uint_as_float(a[1]);
}
#define LDSP(T, a) ((LAS T*)(size_t)(a))

#define STAMP(i) do { asm volatile("s_memrealtime %0\n\ts_waitcnt lgkmcnt(0)" : "=s"(stamps[i]) :: "memory"); } while (0)
#define TS(i)

__device__ __forceinline__ constexpr int b2grp(int s) {
  return s < 3 ? 16 + s : s < 7 ? 8 + s : s < 18 ? s - 7 : s < 21 ? s - 2 : s < 25 ? s - 10 : s < 28 ? s - 25 : s == 28 ? 15 : 19;
}
struct B2Ctx { const char* wo; const char* w2; int a0, rota, f0, nf, rotf; unsigned voff, vhalf, mhalf, mboth; int tb0; };
__device__ __forceinline__ void b2_scalars(B2Ctx& b, const char* wo, const char* w2, unsigned ldsb, int hf, int c, int rt, int ct) {
  b.wo = wo + ct * 128; b.w2 = w2 + ct * 128; b.mboth = ldsb + c * 2048; b.mhalf = b.mboth + hf * 1024;
  b.a0 = 8 * c; b.f0 = c < 3 ? 22 * c : 65; b.nf = c < 2 ? 22 : 21;
  b.rota = ((ct + rt) & 1) * 4; b.rotf = ((ct + rt) & 1) * 10;
}
__device__ __forceinline__ int phys_k16(const B2Ctx& c, int s) {
  if (s < 8) return c.a0 + (s ^ c.rota);
  int t = s - 8 + c.rotf; t = t >= c.nf ? t - c.nf : t;
  return 32 + c.f0 + t;
}
__device__ __forceinline__ const char* b2_src(const B2Ctx& c, int s) {
  return s < 8 ? c.wo + phys_k16(c, s) * 16384 : c.w2 + (phys_k16(c, s) - 32) * 16384;
}
__device__ __forceinline__ void b2_issue(const B2Ctx& c, int s) {
  const char* src = b2_src(c, s);
  set_m0(c.mhalf, b2grp(s) * 8192); nop1();
  glds<0>(c.vhalf, src);
}
__device__ __forceinline__ void b2_issue2(const B2Ctx& c, int s, unsigned voff1) {
  const char* src = b2_src(c, s);
  set_m0(c.mboth, b2grp(s) * 8192); nop1();
  glds<0>(c.voff, src); glds<1024>(voff1, src);
}
struct BRegs { u32x2 lo[10][2], hi[10][2]; };
__device__ __forceinline__ void br_read(BRegs& BR, const LAS char* smem, int tb0, int i) {
  const int s = i >> 2, cb = (i >> 1) & 1;
  if (i & 1) BR.hi[s][cb] = trd(smem + b2grp(s) * 8192 + (tb0 ^ (cb * 64)) + 512); else BR.lo[s][cb] = trd(smem + b2grp(s) * 8192 + (tb0 ^ (cb * 64)));
}

struct Ld { unsigned vo[6]; const char* base0; unsigned mbase; };
struct Epi { f32x4 mp, mc, mp1; unsigned selA, selB; float NEG; float mu[5], rs[5], csv; f32x4 mu4[5], rs4[5], cs[3]; u32x2 b1v[3]; };

template <bool H0, bool QKV, int NS>
struct P1 {
  static constexpr int NRB = H0 ? 5 : 4;
  static constexpr int NL = H0 ? (QKV ? 5 : 4) : 6;
  static constexpr int KSTRIDE = H0 ? 128 : QKV ? 64 * 3072 : 64 * 2752;
  struct Frag { u32x4 A[NRB]; u32x2 Bl[3], Bh[3]; };
  Ld ld; int rot, hdelta; unsigned mbaseh; u64 hmask;
  const char* cb;
  unsigned pb, pa0, pa1;
  u32x4 ones;

  __device__ __forceinline__ void open(int ch) { cb = ld.base0 + ((ch + rot) & 7) * KSTRIDE; set_m0(ld.mbase, (ch % 3) * SLOT); }
  template <int I> __device__ __forceinline__ void ld1(int ch) const {
    if (I >= NL) return;
    if (H0 && I == 4) { set_m0(mbaseh, (ch % 3) * SLOT); glds_mask(ld.vo[4], cb + hdelta, hmask); return; }
    glds<(H0 ? I * 1024 : I * 1024 - 3072)>(ld.vo[I], cb);
  }
  __device__ __forceinline__ void load1(int ch, int i) {
    if (i == 0) { open(ch); nop1(); ld1<0>(ch); } else if (i == 1) ld1<1>(ch); else if (i == 2) ld1<2>(ch);
    else if (i == 3) ld1<3>(ch); else if (i == 4) ld1<4>(ch); else ld1<5>(ch);
  }
  __device__ __forceinline__ void rd(Frag& f, int ks, int n) const {
    constexpr int ord[11] = {0, 1, 6, 2, 3, 4, 5, 7, 8, 9, 10};
    if (n >= 11) return;
    const int i = ord[n];
    if (i < 6) {
      const int s = i >> 1;
      if (s >= NS) return;
      if (i & 1) f.Bh[s] = trd(LDSP(const char, pb) + (32 * ks + 4) * 384 + s * 128); else f.Bl[s] = trd(LDSP(const char, pb) + (32 * ks) * 384 + s * 128);
    } else if (i - 6 < NRB) {
      const int r = H0 ? (i - 5) % 5 : i - 6;
      if (!(H0 && !QKV && r == 0)) f.A[r] = *(const LAS u32x4*)(LDSP(const char, ks ? pa1 : pa0) + r * 2048);
    }
  }
  __device__ __forceinline__ void advance(int delta) {
    asm volatile("v_add_u32 %0, %3, %0\n\tv_add_u32 %1, %3, %1\n\tv_add_u32 %2, %3, %2" : "+v"(pb), "+v"(pa0), "+v"(pa1) : "i"(delta));
  }
  template <class F> __device__ __forceinline__ void mma(f32x4 (&acc)[5][3], Frag& f, F&& fill) const {
#pragma unroll
    for (int rr = 0; rr < NRB; rr++) {
      const int r = H0 ? (rr + 1) % 5 : rr;
#pragma unroll
      for (int s = 0; s < 3; s++) {
        if (s < NS) {
          const u32x4 B = {f.Bl[s].x, f.Bl[s].y, f.Bh[s].x, f.Bh[s].y};
          const u32x4 A = (H0 && !QKV && r == 0) ? ones : f.A[r];
          if (QKV && s == 2) acc[r][s] = __builtin_amdgcn_mfma_f32_16x16x32_bf16(b8(A), b8(B), acc[r][s], 0, 0, 0);
          else acc[r][s] = __builtin_amdgcn_mfma_f32_16x16x32_bf16(b8(B), b8(A), acc[r][s], 0, 0, 0);
          SB();
        }
        fill(3 * rr + s);
        SB();
      }
    }
  }
  template <class F> __device__ __forceinline__ void stepA(f32x4 (&acc)[5][3], Frag& cur, Frag& nx, int delta, F&& site) {
    mma(acc, cur, [&](int n) {
      rd(nx, 1, n);
      constexpr int S0 = H0 ? 4 : 2;
      if (n >= S0 && (n - S0) % 4 == 0 && (n - S0) / 4 < 3) site((n - S0) / 4);
      if (n == 3 * NRB - 2 && delta != 0) advance(delta);
    });
  }
  template <class S, class F> __device__ __forceinline__ void stepB(f32x4 (&acc)[5][3], Frag& cur, Frag& nx, S&& sync, F&& site) const {
    mma(acc, cur, [&](int n) {
      if (n == BG - 1) sync();
      if (n >= BG - 1) { const int i = n - (BG - 1); if (i < 2) { rd(nx, 0, 2 * i); rd(nx, 0, 2 * i + 1); } else rd(nx, 0, i + 2); }
      constexpr int S0 = H0 ? 6 : 5, SD = H0 ? 4 : 3;
      if (n >= S0 && (n - S0) % SD == 0 && (n - S0) / SD < 3) site((n - S0) / SD);
    });
  }
};

template <bool H0, bool QKV, int NSB>
__device__ __forceinline__ void phase1(const Params& P, LAS char* smem, unsigned ldsb, const Ld& ld, int p, int j, int c, int rot, int rt, int ct,
                                       B2Ctx& b2c, f32x4 (&acc)[5][3], Epi& ep, u64& tag, u64 (&stamps)[NSTAMP], unsigned& tsv) {
  typedef P1<H0, QKV, NSB> T;
  constexpr int NL = T::NL, HF = H0 ? 0 : 1;
  const int lane = threadIdx.x & 63;
  T t;
  t.ld = ld; t.rot = rot;
  t.mbaseh = ldsb + 4 * c * 128; t.hdelta = p > 0 ? -16384 : 0; t.hmask = c == 0 ? 0xffff0000ull : 0xffffffffull;
  u64 e_old;
  {
    const int g = lane >> 4, m = lane & 15, q4 = m >> 2, pp = lane & 3;
    t.pa0 = ldsb + (H0 ? 0 : 5 * 2048) + m * 128 + ((g ^ (m >> 1)) * 16); t.pa1 = t.pa0 ^ 64;
    t.pb = ldsb + XB + (8 * g + q4) * 384 + (((2 * c + (pp >> 1)) ^ (2 * ((q4 >> 1) | ((g & 1) << 1)))) * 16) + (pp & 1) * 8;
    const unsigned o = m == 0 ? ONES : 0u; t.ones = (u32x4){o, o, o, o};
    if (H0 && !QKV) asm volatile("" : "+v"(t.ones));
    if (H0 && QKV) { if (c == 0 && lane < 48) *(LAS u32x4*)(smem + g * SLOT + m * 16) = (u32x4){ONES, ONES, ONES, ONES}; }
  }
#pragma unroll
  for (int r = 0; r < 5; r++)
#pragma unroll
    for (int s = 0; s < 3; s++) acc[r][s] = (f32x4){0.f, 0.f, 0.f, 0.f};
  typename T::Frag F0, F1;
  constexpr int NX = H0 ? 2 : 0, NSS = H0 ? 1 : 0;
  const int srow = p * 128 + ((QKV ? 0 : 8) + j) * 8 + 2 * c;
  u32x4 xr0, xr1; float xv[16], mean, sq; unsigned ssa;
  auto row_stats = [&](int k) {
    if (k == 0) {
#pragma unroll
      for (int e = 0; e < 4; e++) { xv[2 * e] = bf_lo(xr0[e]); xv[2 * e + 1] = bf_hi(xr0[e]); xv[8 + 2 * e] = bf_lo(xr1[e]); xv[9 + 2 * e] = bf_hi(xr1[e]); }
      float s0 = xv[0], s1 = xv[8];
#pragma unroll
      for (int e = 1; e < 8; e++) { s0 = add_f32(s0, xv[e]); s1 = add_f32(s1, xv[8 + e]); }
      mean = s0 + s1;
      asm volatile("" : "+v"(mean));
    }
    if (k == 1) { mean = half_sum(mean) * (1.f / 512.f); asm volatile("" : "+v"(mean)); }
    if (k == 2) {
      float q0 = 0.f, q1 = 0.f;
#pragma unroll
      for (int e = 0; e < 8; e++) { float d = sub_f32(xv[e], mean), d2 = sub_f32(xv[8 + e], mean); q0 = fmaf(d, d, q0); q1 = fmaf(d2, d2, q1); asm volatile("" : "+v"(q0), "+v"(q1)); }
      sq = q0 + q1;
      asm volatile("" : "+v"(sq));
    }
    if (k == 3) { sq = __builtin_amdgcn_rsqf(half_sum(sq) * (1.f / 512.f) + 1e-5f); asm volatile("" : "+v"(sq)); }
    if (k == 4) {
      const u32x2 gr = {__float_as_uint((lane & 1) ? sq : mean), (unsigned)tag};
      asm volatile("s_mov_b64 exec, %3\n\tglobal_store_dwordx2 %0, %1, %2 sc1\n\ts_mov_b64 exec, -1" :: "v"(ssa), "v"(gr), "s"(P.ws + WS_STAT), "s"(0x0000000300000003ull) : "memory");
    }
  };
  if (H0) {
    if (!XF) asm volatile("global_load_dwordx4 %0, %2, %3\n\tglobal_load_dwordx4 %1, %2, %3 offset:512" : "=&v"(xr0), "=&v"(xr1) : "v"((lane >> 5) * 1024 + (lane & 31) * 16), "s"(P.x + srow * 1024) : "memory");
    ssa = (srow + (lane >> 5)) * 16 + (lane & 1) * 8; asm volatile("" : "+v"(ssa));
  }
  asm volatile("s_load_dwordx2 %0, %1, 0x0" : "=s"(e_old) : "s"(P.ws) : "memory");
  unsigned pfd = 0, pfo; const char* pfw;
  auto warm = [&](int k) {
    asm volatile("s_mov_b64 exec, %3\n\tglobal_load_dword %0, %1, %2\n\ts_mov_b64 exec, -1" : "+v"(pfd) : "v"(pfo), "s"(pfw), "s"(0x3ull << (2 * k)) : "memory");
  };
  unsigned sto, stw, str1, str4, csw, csw1, kvw, vq[4];
  const char *bA, *bB, *bC;
  auto later = [&](int k) {
    const int g = lane >> 4, m = lane & 15, w = 4 * HF + c;
    if (k == 0) {
      u64 z = mix64(e_old); unsigned lo = __builtin_amdgcn_readfirstlane((unsigned)z), hi = __builtin_amdgcn_readfirstlane((unsigned)(z >> 32));
      asm volatile("" : "+s"(lo), "+s"(hi)); tag = ((u64)hi << 32) | lo;
    }
    if (k == 1) {
      const int l0 = blockIdx.x * 59 + w * 8, lim = blockIdx.x * 59 + 58 - l0;
      const int n0 = 4096 - l0;
      pfw = l0 < 4096 ? P.wo + l0 * 128 : P.w2 + (l0 - 4096) * 128;
      const int top = (l0 < 4096 && n0 - 1 < lim) ? n0 - 1 : lim;
      const int l = lane & 7;
      pfo = (l < top ? l : top) * 128; asm volatile("" : "+v"(pfo), "+s"(pfw));
      const int sti = 18 * w + (lane < 18 ? lane : 17), row = p * 128 - 16 + sti;
      sto = (row < 0 ? 0 : row) * 16; stw = ldsb + ST_MEAN + sti * 4; str1 = ldsb + ST_MEAN + (64 * HF + m) * 4;
      str4 = QKV ? ldsb + ST_MEAN + (64 * HF + 4 * g) * 4 : ldsb + KVX + (c * 16 + 4 * g) * 2;
      csw = ldsb + ST_CS + (c * 16 + 4 * g) * 4; csw1 = ldsb + ST_CS + (128 + c * 16 + m) * 4; kvw = ldsb + KVX + c * 2048 + lane * 16;
      asm volatile("" : "+v"(sto), "+v"(stw), "+v"(str1), "+v"(str4), "+v"(csw));
      if (QKV) asm volatile("" : "+v"(csw1), "+v"(kvw));
      b2c.voff = (lane >> 3) * 1024 + (((lane & 7) ^ ((g & 1) << 2)) * 16);
#pragma unroll
      for (int i = 0; i < 4; i++) vq[i] = b2c.voff + HF * 8192 + i * 16384;
      b2c.tb0 = c * 2048 + (8 * (g >> 1) + (m >> 2)) * 128 + (((2 * (((g & 1) + c) & 3) + ((lane & 3) >> 1)) ^ ((m >> 3) << 2)) * 16) + (lane & 1) * 8;
      asm volatile("" : "+v"(b2c.voff), "+v"(vq[0]), "+v"(vq[1]), "+v"(vq[2]), "+v"(vq[3]), "+v"(b2c.tb0));
      b2c.vhalf = vq[0];
      if (QKV) {
        constexpr u64 MP[4] = {0x555005500050000ull, 0xaaa00aa000a0000ull, 0x1555015500150001ull, 0x2aaa02aa002a0002ull}, MC[4] = {0x5000550055505555ull, 0xa000aa00aaa0aaaaull, 0x4000540055405554ull, 0x8000a800aa80aaa8ull};
        ep.NEG = -3.0e38f; asm volatile("" : "+v"(ep.NEG));
        const u64 live = (H0 && p == 0) ? 0ull : ~0ull;
#pragma unroll
        for (int e = 0; e < 4; e++) {
          ep.mp[e] = sel0(ep.NEG, MP[e]); ep.mc[e] = sel0(ep.NEG, MC[e]);
          ep.mp1[e] = H0 ? sel0(ep.NEG, MP[e] & live) : ep.mp[e];
        }
        ep.selA = (m & 1) ? 0x01000c0cu : 0x0c0c0100u; ep.selB = ep.selA + ((m & 1) ? 0x02020000u : 0x00000202u);
        asm volatile("" : "+v"(ep.selA), "+v"(ep.selB));
      }
    }
    if (k == 2) {
      b2_scalars(b2c, P.wo, P.w2, ldsb, HF, c, rt, ct);
      bA = b2_src(b2c, 0); asm volatile("" : "+s"(bA), "+s"(b2c.mhalf));
    }
    if (k == 3) { bB = b2_src(b2c, 4); asm volatile("" : "+s"(bB)); }
    if (k == 4) { bC = b2_src(b2c, 8); asm volatile("" : "+s"(bC)); }
    if (k == 5 && H0) { if (lane == 0) *LDSP(unsigned, ldsb + PAIRF + c * 4) = 0u; }
  };
  auto b2_early = [&](int i) { set_m0(b2c.mhalf, b2grp(i) * 8192); nop1(); glds<0>(vq[i & 3], i < 4 ? bA : i < 8 ? bB : bC); };
  later(1);
  SB();
  TS(63);
  if (!H0) {
#pragma unroll
    for (int i = 0; i < 6; i++) t.load1(1, i);
  }
  asm volatile("s_waitcnt lgkmcnt(0)" : "+s"(e_old) :: "memory");
  later(0);
  if (H0 && XF) {
    wait_vm<2 * NL>();
    TS(2);
    { const LAS char* q = smem + 16 * 8192 + c * 2048 + (lane >> 5) * 1024 + (lane & 31) * 16; xr0 = *(const LAS u32x4*)q; xr1 = *(const LAS u32x4*)(q + 512); }
#pragma unroll
    for (int k = 0; k < 5; k++) row_stats(k);
  }
  SB();
  TS(3);
  wait_vm<NL + (XF ? NSS : NX)>();
  TS(4);
  bar();
  TS(5);
  STAMP(1);
#pragma unroll
  for (int n = 0; n < 11; n++) t.rd(F0, 0, n);
  t.stepA(acc, F0, F1, SLOT, [&](int k) { t.load1(2, 2 * k); t.load1(2, 2 * k + 1); });
  t.stepB(acc, F1, F0, [&]() { TS(6); wait_vm<NL + (XF ? NSS : NX)>(); TS(7); bar(); TS(8); }, [&](int k) { t.load1(3, k); });
#pragma unroll
  for (int ch = 1; ch < 5; ch++) {
    t.stepA(acc, F0, F1, ch % 3 == 2 ? -2 * SLOT : SLOT, [&](int k) {
      if (H0 && !XF && ch == 2 && k == 0) row_stats(3);
      t.load1(ch + 2, 3 + k);
      if (k == 2) { warm(ch - 1); if (H0 && !XF && ch == 2) row_stats(4); }
    });
    t.stepB(acc, F1, F0, [&]() {
      TS(6 + 3 * ch);
      if (ch == 1) { if (H0 && !XF) asm volatile("s_waitcnt vmcnt(%2) lgkmcnt(0)" : "+v"(xr0), "+v"(xr1) : "i"(NL + 1) : "memory"); else wait_vm<NL + 1>(); }
      else if (ch < 4) wait_vm<NL + 2 + (XF ? 0 : NSS)>();
      else wait_vm<NL + 2>();
      TS(7 + 3 * ch); bar(); TS(8 + 3 * ch);
    }, [&](int k) {
      t.load1(ch + 3, k);
      if (H0 && !XF && ch == 1) row_stats(k);
      if (k == 1) later(1 + ch);
    });
  }
  u32x4 st;
  t.stepA(acc, F0, F1, -2 * SLOT, [&](int k) { t.load1(7, 3 + k); });
  t.stepB(acc, F1, F0, [&]() {
    TS(21); asm volatile("s_waitcnt vmcnt(%1) lgkmcnt(0)" : "+v"(pfd) : "i"(NL) : "memory"); TS(22); bar(); TS(23);
    asm volatile("global_load_dwordx4 %0, %1, %2 sc1" : "=v"(st) : "v"(sto), "s"(P.ws + WS_STAT) : "memory");
  }, [&](int k) { b2_early(k); });
  t.stepA(acc, F0, F1, SLOT, [&](int k) { b2_early(3 + k); });
  t.stepB(acc, F1, F0, [&]() { TS(24); wait_vm<7>(); TS(25); bar(); TS(26); }, [&](int k) { b2_early(6 + k); });
  t.stepA(acc, F0, F1, 0, [&](int k) { b2_early(9 + k); });
  TS(27);
  asm volatile("s_waitcnt vmcnt(12)" : "+v"(st) :: "memory");
  t.mma(acc, F1, [&](int n) {
    if (n == 1) {
      const unsigned tg = (unsigned)tag;
      if (!__all(st.y == tg && st.w == tg)) {
        const GAS u64* sp = (const GAS u64*)(P.ws + WS_STAT + sto);
#pragma unroll 1
        for (unsigned spin = 0; spin < SPIN_CAP; spin++) {
          u64 a = __hip_atomic_load(sp, __ATOMIC_RELAXED, __HIP_MEMORY_SCOPE_AGENT), b = __hip_atomic_load(sp + 1, __ATOMIC_RELAXED, __HIP_MEMORY_SCOPE_AGENT);
          st = (u32x4){(unsigned)a, (unsigned)(a >> 32), (unsigned)b, (unsigned)(b >> 32)};
          if (__all(st.y == tg && st.w == tg)) break;
          __builtin_amdgcn_s_sleep(1);
        }
      }
      *LDSP(unsigned, stw) = st.x; *LDSP(unsigned, stw + 576) = st.z;
    }
  });
  STAMP(2);
  if (H0) {
    if (QKV) {
      *LDSP(f32x4, kvw) = acc[4][1];
      *LDSP(f32x4, kvw + 1024) = acc[4][2];
      if (lane < 16) *LDSP(float, csw1) = acc[0][2][0];
    }
    if ((lane & 15) == 0) {
      *LDSP(f32x4, csw) = acc[0][0];
      *LDSP(f32x4, csw + 256) = acc[0][1];
      if (!QKV) *LDSP(f32x4, csw + 512) = acc[0][2];
    }
  } else {
#pragma unroll
    for (int r = 4; r >= 1; r--)
#pragma unroll
      for (int s = 0; s < 3; s++) acc[r][s] = acc[r - 1][s];
  }
  TS(29);
  wait_vm<12 - NE>();
  if (H0) __builtin_amdgcn_s_setprio(2); else __builtin_amdgcn_s_setprio(1);
  bar();
  TS(31);
  ep.cs[0] = *LDSP(const f32x4, csw); ep.cs[1] = *LDSP(const f32x4, csw + 256);
  if (QKV) ep.csv = *LDSP(const float, csw1); else ep.cs[2] = *LDSP(const f32x4, csw + 512);
  if (!H0 && QKV) { acc[0][1] = *LDSP(const f32x4, kvw); acc[0][2] = *LDSP(const f32x4, kvw + 1024); }
#pragma unroll
  for (int r = QKV ? 0 : 1; r < 5; r++) {
    ep.mu[r] = *LDSP(const float, str1 + 64 * r); ep.rs[r] = *LDSP(const float, str1 + 64 * r + 576);
    if (QKV) { ep.mu4[r] = *LDSP(const f32x4, str4 + 64 * r); ep.rs4[r] = *LDSP(const f32x4, str4 + 64 * r + 576); }
    else if (r < 4) ep.b1v[r - 1] = *LDSP(const u32x2, str4 + 128 * (r - 1));
  }
  SB();
}

typedef __attribute__((ext_vector_type(16))) unsigned u32x16;
__device__ __forceinline__ char* mkptr(unsigned lo, unsigned hi) { return (char*)(((u64)hi << 32) | lo); }
__global__ __launch_bounds__(512, 2) void fused_block(Params) {
  u64 stamps[NSTAMP];
  unsigned tsv = 0;
  TS(0);
  STAMP(0);
  u32x16 ka; u32x2 kb;
  asm volatile("s_load_dwordx16 %0, %2, 0x0\n\ts_load_dwordx2 %1, %2, 0x40" : "=&s"(ka), "=&s"(kb) : "s"(__builtin_amdgcn_kernarg_segment_ptr()));
  Params P;
  auto args = [&]() {
    asm volatile("s_waitcnt lgkmcnt(0)" : "+s"(ka), "+s"(kb));
    P.x = mkptr(ka[0], ka[1]); P.wqkv = mkptr(ka[2], ka[3]); P.wo = mkptr(ka[4], ka[5]); P.w1 = mkptr(ka[6], ka[7]); P.b1 = mkptr(ka[8], ka[9]);
    P.w2 = mkptr(ka[10], ka[11]); P.b2 = mkptr(ka[12], ka[13]); P.out = mkptr(ka[14], ka[15]); P.ws = mkptr(kb[0], kb[1]);
  };
  __shared__ __attribute__((aligned(16))) char smem_[163840];
  LAS char* smem = (LAS char*)smem_;
  const unsigned ldsb = (unsigned)(size_t)smem;
  const int tid = threadIdx.x, lane = tid & 63, wave = __builtin_amdgcn_readfirstlane(tid >> 6);
  const int b = blockIdx.x, xcd = b & 7, r5 = b >> 3;
  unsigned cpd = 0;
  if (CPF == 1) {
    u64 pc; asm volatile("s_getpc_b64 %0" : "=s"(pc)); pc &= ~255ull;
    asm volatile("global_load_dword %0, %1, %2" : "+v"(cpd) : "v"(((tid < CODE_LINES / 32 ? tid : 0) * 32 + r5) * 128), "s"(pc) : "memory");
  }
  const int hf = wave >> 2, c = wave & 3, jj = r5 >> 3;
  const bool qkv = jj < 2;
  const int p = (xcd & 1) * 8 + (r5 & 7), j = (xcd >> 1) * 2 + (jj & 1);
  const int rot1 = ROT1_MODE == 0 ? 0 : ROT1_MODE == 1 ? (p + jj) : ROT1_MODE == 2 ? ((p + jj) & 1) * 4 : ((p + jj) & 3) * 2;
  const int rt = xcd * 4 + jj, ct = r5 & 7;
  Ld ld;
  const int pitch = qkv ? 3072 : 2752, k0 = rot1 & 7, k1 = (rot1 + 1) & 7;
  int so0, ko0, ko1;
  if (hf == 0) {
    so0 = p * 131072 - 4096; ko0 = k0 * 128; ko1 = k1 * 128; ld.mbase = ldsb + (16 + 32 * c) * 128;
    asm volatile("" : "+s"(so0), "+s"(ko0), "+s"(ko1), "+s"(ld.mbase));
    const unsigned v0 = 4096 + (32 * c + (lane >> 3)) * 1024 + (((lane & 7) ^ (lane >> 4)) * 16);
    ld.vo[0] = v0; ld.vo[1] = (v0 ^ 64) + 7168; ld.vo[2] = v0 + 14336; ld.vo[3] = (v0 ^ 64) + 21504;
    { const int row = 4 * c + ((lane >> 3) & 3); ld.vo[4] = 4096 + row * 1024 + (((lane & 7) ^ (row >> 1)) * 16); }
    ld.vo[5] = (lane < (j == 7 ? 18 : 22) ? lane : 0) * 16;
  } else {
    const int secst = qkv ? 1024 : 128, lim = qkv ? 23 : (j == 7 ? 17 : 21);
    so0 = j * (qkv ? 128 : 352) + c * 16 * pitch - 4096; ko0 = k0 * 64 * pitch; ko1 = k1 * 64 * pitch; ld.mbase = ldsb + XB + c * 6144 + 3072;
    asm volatile("" : "+s"(so0), "+s"(ko0), "+s"(ko1), "+s"(ld.mbase));
#pragma unroll
    for (int r = 0; r < 3; r++) {
      const int v = lane + 64 * r, row = (v * 171) >> 12, cc = (v - 24 * row) ^ (row & 2);
      const int c0 = cc < lim ? cc : lim, c1 = (cc ^ 4) < lim ? (cc ^ 4) : lim;
      ld.vo[r] = 4096 + 3072 - 1024 * r + row * pitch + (c0 >> 3) * secst + (c0 & 7) * 16;
      ld.vo[r + 3] = 4096 - 1024 * r + (row + 8) * pitch + (c1 >> 3) * secst + (c1 & 7) * 16;
    }
  }
  unsigned xvo = lane * 16;
  asm volatile("" : "+v"(ld.vo[0]), "+v"(ld.vo[1]), "+v"(ld.vo[2]), "+v"(ld.vo[3]), "+v"(ld.vo[4]), "+v"(ld.vo[5]), "+v"(xvo));
  SB();
  args();
  ld.base0 = (hf == 0 ? P.x : qkv ? P.wqkv : P.w1) + so0;
  TS(62);
  if (hf == 0) {
    const u64 hmask = c == 0 ? 0xffff0000ull : 0xffffffffull;
    if (XF) {
      const char* xrow = P.x + (p * 128 + ((qkv ? 0 : 8) + j) * 8 + 2 * c) * 1024;
      set_m0(ldsb + c * 2048, 16 * 8192); nop1(); glds<0>(xvo, xrow); glds<1024>(xvo, xrow);
    }
#pragma unroll
    for (int ch = 0; ch < 2; ch++) {
      const char* cb = ld.base0 + (ch ? ko1 : ko0);
      set_m0(ld.mbase, ch * SLOT); nop1();
      glds<0>(ld.vo[0], cb); glds<1024>(ld.vo[1], cb); glds<2048>(ld.vo[2], cb); glds<3072>(ld.vo[3], cb);
      if (qkv) { set_m0(ldsb + 4 * c * 128, ch * SLOT); glds_mask(ld.vo[4], cb + (p > 0 ? -16384 : 0), hmask); }
    }
    if (!qkv && c == 0) { set_m0(ldsb, KVX); nop1(); glds<0>(ld.vo[5], P.b1 + j * 352); }
  } else {
    const char* cb = ld.base0 + ko0;
    set_m0(ld.mbase, 0); nop1();
    glds<-3072>(ld.vo[0], cb); glds<-2048>(ld.vo[1], cb); glds<-1024>(ld.vo[2], cb); glds<0>(ld.vo[3], cb); glds<1024>(ld.vo[4], cb); glds<2048>(ld.vo[5], cb);
  }
  SB();
  TS(1);
  B2Ctx b2c; f32x4 acc[5][3]; u64 tag; Epi ep;
#define P1ARGS P, smem, ldsb, ld, p, j, c, rot1, rt, ct, b2c, acc, ep, tag, stamps, tsv
  const bool blk2 = 8 + c < ((j == 7) ? 9 : 11);
  if (qkv) { if (hf == 0) phase1<true, true, 3>(P1ARGS); else phase1<false, true, 3>(P1ARGS); }
  else if (blk2) { if (hf == 0) phase1<true, false, 3>(P1ARGS); else phase1<false, false, 3>(P1ARGS); }
  else { if (hf == 0) phase1<true, false, 2>(P1ARGS); else phase1<false, false, 2>(P1ARGS); }
  STAMP(3);
  asm volatile("" :: "v"(cpd));
  b2_scalars(b2c, P.wo, P.w2, ldsb, hf, c, rt, ct);
  const __amdgpu_buffer_rsrc_t act = __builtin_amdgcn_make_buffer_rsrc(P.ws + WS_ACT, 0, ACT_BYTES, 0x00020000);
  const int rt32 = p * 4 + hf * 2, tb0 = b2c.tb0;
  BRegs BR;
  auto side = [&](int u) {
    SB();
    b2_issue(b2c, 12 + u);
    if (hf == 1) {
      if (u < 4) {
#pragma unroll
        for (int i = 0; i < NE; i++) br_read(BR, smem, tb0, NE * u + i);
      }
      if (u == 5) { wait_lgkm0(); if (lane == 0) *(LAS unsigned*)(smem + PAIRF + c * 4) = 1u; }
    }
    SB();
  };
  if (qkv) {
    u32x2 qf[5], kf[5], vf[5];
#pragma unroll
    for (int r = 0; r < 5; r++) {
      f32x4 k4 = (acc[r][1] - ep.mu[r] * ep.cs[1]) * ep.rs[r], v4 = (acc[r][2] - ep.mu4[r] * ep.csv) * ep.rs4[r];
      kf[r] = (u32x2){pk_bf16(k4[0], k4[1]), pk_bf16(k4[2], k4[3])};
      vf[r] = (u32x2){pk_bf16(v4[0], v4[1]), pk_bf16(v4[2], v4[3])};
      if (r > 0) { f32x4 q4v = (acc[r][0] - ep.mu[r] * ep.cs[0]) * ep.rs[r]; qf[r] = (u32x2){pk_bf16(q4v[0], q4v[1]), pk_bf16(q4v[2], q4v[3])}; }
      side(r);
    }
    TS(32);
    const float SC = 0.25f * 1.4426950408889634f, NEG = ep.NEG;
    float sv[4][4], mx[4];
#pragma unroll
    for (int r = 1; r < 5; r++) {
      f32x4 sp = __builtin_amdgcn_mfma_f32_16x16x16bf16_1k(__builtin_bit_cast(s16x4, kf[r - 1]), __builtin_bit_cast(s16x4, qf[r]), r == 1 ? ep.mp1 : ep.mp, 0, 0, 0);
      f32x4 sc = __builtin_amdgcn_mfma_f32_16x16x16bf16_1k(__builtin_bit_cast(s16x4, kf[r]), __builtin_bit_cast(s16x4, qf[r]), ep.mc, 0, 0, 0);
      sv[r - 1][0] = max3f(sp[0], sp[1], NEG); sv[r - 1][1] = max3f(sp[2], sp[3], NEG); sv[r - 1][2] = max3f(sc[0], sc[1], NEG); sv[r - 1][3] = max3f(sc[2], sc[3], NEG);
      mx[r - 1] = max3f(max3f(sv[r - 1][0], sv[r - 1][1], sv[r - 1][2]), sv[r - 1][3], NEG);
    }
    side(5);
    TS(36);
    {
      auto a = __builtin_amdgcn_permlane16_swap(__float_as_uint(mx[0]), __float_as_uint(mx[1]), false, false);
      auto b = __builtin_amdgcn_permlane16_swap(__float_as_uint(mx[2]), __float_as_uint(mx[3]), false, false);
      const float u = max3f(__uint_as_float(a[0]), __uint_as_float(a[1]), NEG), v = max3f(__uint_as_float(b[0]), __uint_as_float(b[1]), NEG);
      auto d = __builtin_amdgcn_permlane32_swap(__float_as_uint(u), __float_as_uint(v), false, false);
      const unsigned t = __float_as_uint(max3f(__uint_as_float(d[0]), __uint_as_float(d[1]), NEG) * SC);
      auto e = __builtin_amdgcn_permlane32_swap(t, t, false, false);
      auto f0 = __builtin_amdgcn_permlane16_swap(e[0], e[0], false, false);
      auto f1 = __builtin_amdgcn_permlane16_swap(e[1], e[1], false, false);
      mx[0] = __uint_as_float(f0[0]); mx[1] = __uint_as_float(f0[1]); mx[2] = __uint_as_float(f1[0]); mx[3] = __uint_as_float(f1[1]);
    }
    const u32x4 one4 = {ONES, ONES, ONES, ONES};
    const f32x4 z = {0.f, 0.f, 0.f, 0.f};
    f32x4 o4[4], l4[4];
#pragma unroll
    for (int r = 0; r < 4; r++) {
      float e[4];
#pragma unroll
      for (int i = 0; i < 4; i++) e[i] = __builtin_amdgcn_exp2f(sv[r][i] * SC - mx[r]);
      const unsigned pa = pk_bf16(e[0], e[1]), pb = pk_bf16(e[2], e[3]);
      const u32x4 pf = {__builtin_amdgcn_perm(0u, pa, ep.selA), __builtin_amdgcn_perm(0u, pa, ep.selB), __builtin_amdgcn_perm(0u, pb, ep.selA), __builtin_amdgcn_perm(0u, pb, ep.selB)};
      const u32x4 vv = {vf[r].x, vf[r].y, vf[r + 1].x, vf[r + 1].y};
      o4[r] = __builtin_amdgcn_mfma_f32_16x16x32_bf16(b8(vv), b8(pf), z, 0, 0, 0);
      l4[r] = __builtin_amdgcn_mfma_f32_16x16x32_bf16(b8(one4), b8(pf), z, 0, 0, 0);
    }
    u32x2 of[4];
#pragma unroll
    for (int r = 0; r < 4; r++) {
      const f32x4 o = o4[r] * __builtin_amdgcn_rcpf(l4[r][0]);
      of[r] = (u32x2){pk_bf16(o[0], o[1]), pk_bf16(o[2], o[3])};
    }
#pragma unroll
    for (int pr = 0; pr < 2; pr++) {
      auto s0 = __builtin_amdgcn_permlane16_swap(of[2 * pr].x, of[1 + 2 * pr].x, false, false);
      auto s1 = __builtin_amdgcn_permlane16_swap(of[2 * pr].y, of[1 + 2 * pr].y, false, false);
      __builtin_amdgcn_raw_buffer_store_b128((u32x4){s0[0], s1[0], s0[1], s1[1]}, act, lane * 16 + ((rt32 + pr) * NK16 + 4 * j + c) * 1024, 0, 16);
    }
    SB();
  } else {
    auto gelu_block = [&](int s) {
      const f32x4 bb = {bf_lo(ep.b1v[s].x), bf_hi(ep.b1v[s].x), bf_lo(ep.b1v[s].y), bf_hi(ep.b1v[s].y)};
      u32x2 hfr[4];
#pragma unroll
      for (int r = 0; r < 4; r++) {
        if (4 * s + r < 6) side(4 * s + r);
        f32x4 v = (acc[r + 1][s] - ep.mu[r + 1] * ep.cs[s]) * ep.rs[r + 1] + bb;
        f32x4 zz = v * (v * v * (-0.10294324f) + (-2.3022082f));
        f32x4 ee = {__builtin_amdgcn_exp2f(zz[0]), __builtin_amdgcn_exp2f(zz[1]), __builtin_amdgcn_exp2f(zz[2]), __builtin_amdgcn_exp2f(zz[3])};
        ee = ee + 1.f;
        f32x4 h = v * (f32x4){__builtin_amdgcn_rcpf(ee[0]), __builtin_amdgcn_rcpf(ee[1]), __builtin_amdgcn_rcpf(ee[2]), __builtin_amdgcn_rcpf(ee[3])};
        hfr[r] = (u32x2){pk_bf16(h[0], h[1]), pk_bf16(h[2], h[3])};
        if (r & 1) {
          const int pr = r >> 1;
          auto s0 = __builtin_amdgcn_permlane16_swap(hfr[2 * pr].x, hfr[2 * pr + 1].x, false, false);
          auto s1 = __builtin_amdgcn_permlane16_swap(hfr[2 * pr].y, hfr[2 * pr + 1].y, false, false);
          __builtin_amdgcn_raw_buffer_store_b128((u32x4){s0[0], s1[0], s0[1], s1[1]}, act, lane * 16 + ((rt32 + pr) * NK16 + 32 + 11 * j + 4 * s + c) * 1024, 0, 16);
        }
      }
      SB();
      TS(32 + s);
    };
    gelu_block(0);
    gelu_block(1);
    if (blk2) gelu_block(2);
  }
  STAMP(4);
  TS(40);
  if (hf == 0) {
    __builtin_amdgcn_s_setprio(0);
#pragma unroll
    for (int i = 0; i < 4 * NE; i++) br_read(BR, smem, tb0, i);
#pragma unroll 1
    for (unsigned spin = 0; spin < SPIN_CAP; spin++)
      if (__builtin_amdgcn_readfirstlane(__hip_atomic_load((LAS unsigned*)(smem + PAIRF + c * 4), __ATOMIC_RELAXED, __HIP_MEMORY_SCOPE_WORKGROUP)) == 1u) break;
    wait_lgkm0();
#pragma unroll
    for (int s = 18; s < 18 + NE; s++) b2_issue2(b2c, s, b2c.voff + 7168);
    TS(44);
    wait_vm<2 * NE>();
  } else {
    wait_vm<0>();
    __builtin_amdgcn_s_setprio(0);
  }
  TS(41);
  bar();
  if (tid == 0) __hip_atomic_store((GAS u64*)(P.ws + WS_FLAG + p * 128 + (qkv ? j : 8 + j) * 8), tag, __ATOMIC_RELAXED, __HIP_MEMORY_SCOPE_AGENT);
  STAMP(5);
  TS(42);
#pragma unroll
  for (int i = 4 * NE; i < 40; i++) br_read(BR, smem, tb0, i);
  wait_lgkm0();
  bar();
#pragma unroll
  for (int s = 18 + NE; s < 30; s++) b2_issue(b2c, s);
  const int orow = rt * 64 + hf * 32 + (lane & 31), ocol = ct * 64 + c * 16 + (lane >> 5) * 4;
  const u32x2 xs0 = *(const GAS u32x2*)(P.x + orow * 1024 + ocol * 2), xs1 = *(const GAS u32x2*)(P.x + orow * 1024 + ocol * 2 + 16);
  const u32x2 bv0 = *(const GAS u32x2*)(P.b2 + ocol * 2), bv1 = *(const GAS u32x2*)(P.b2 + ocol * 2 + 16);
  TS(45);
  if (wave == 0) {
    if (b == 0) {
      const GAS u64* fa = (const GAS u64*)(P.ws + WS_FLAG) + lane;
#pragma unroll 1
      for (unsigned spin = 0; spin < SPIN_CAP; spin++) {
        u64 v0 = __hip_atomic_load(fa, __ATOMIC_RELAXED, __HIP_MEMORY_SCOPE_AGENT), v1 = __hip_atomic_load(fa + 64, __ATOMIC_RELAXED, __HIP_MEMORY_SCOPE_AGENT);
        u64 v2 = __hip_atomic_load(fa + 128, __ATOMIC_RELAXED, __HIP_MEMORY_SCOPE_AGENT), v3 = __hip_atomic_load(fa + 192, __ATOMIC_RELAXED, __HIP_MEMORY_SCOPE_AGENT);
        if (__all(v0 == tag && v1 == tag && v2 == tag && v3 == tag)) break;
        __builtin_amdgcn_s_sleep(1);
      }
      if (lane == 0) __hip_atomic_store((GAS u64*)P.ws, tag, __ATOMIC_RELAXED, __HIP_MEMORY_SCOPE_AGENT);
    } else {
      const GAS u64* fl = (const GAS u64*)(P.ws + WS_FLAG + (rt >> 1) * 128 + (lane & 15) * 8);
#pragma unroll 1
      for (unsigned spin = 0; spin < SPIN_CAP; spin++) {
        if (__all(__hip_atomic_load(fl, __ATOMIC_RELAXED, __HIP_MEMORY_SCOPE_AGENT) == tag)) break;
        __builtin_amdgcn_s_sleep(1);
      }
    }
  }
  TS(49);
  bar();
  TS(50);
  STAMP(6);
  const int abase = (rt * 2 + hf) * NK16;
  auto aload = [&](int s) -> u32x4 {
    const int vo = (s == 29 && b2c.nf == 21) ? 0x7f000000 : lane * 16;
    return __builtin_amdgcn_raw_buffer_load_b128(act, vo, (abase + phys_k16(b2c, s)) * 1024, 16);
  };
  u32x4 AF[PF];
  f32x16 o2[2];
#pragma unroll
  for (int e = 0; e < 16; e++) { o2[0][e] = 0.f; o2[1][e] = 0.f; }
#pragma unroll
  for (int s = 0; s < PF; s++) AF[s] = aload(s);
#pragma unroll
  for (int s = 0; s < 30; s++) {
    if (s == 18) { wait_lgkm0(); bar(); }
    u32x4 B0, B1;
    if (s < 10) { B0 = (u32x4){BR.lo[s][0].x, BR.lo[s][0].y, BR.hi[s][0].x, BR.hi[s][0].y}; B1 = (u32x4){BR.lo[s][1].x, BR.lo[s][1].y, BR.hi[s][1].x, BR.hi[s][1].y}; }
    else {
      u32x2 lo = trd(smem + b2grp(s) * 8192 + tb0), hi = trd(smem + b2grp(s) * 8192 + tb0 + 512);
      B0 = (u32x4){lo.x, lo.y, hi.x, hi.y};
      lo = trd(smem + b2grp(s) * 8192 + (tb0 ^ 64)); hi = trd(smem + b2grp(s) * 8192 + (tb0 ^ 64) + 512);
      B1 = (u32x4){lo.x, lo.y, hi.x, hi.y};
    }
    const u32x4 a = AF[s % PF];
    o2[0] = __builtin_amdgcn_mfma_f32_32x32x16_bf16(b8(B0), b8(a), o2[0], 0, 0, 0);
    o2[1] = __builtin_amdgcn_mfma_f32_32x32x16_bf16(b8(B1), b8(a), o2[1], 0, 0, 0);
    if (s + PF < 30) AF[s % PF] = aload(s + PF);
    SB();
    if (s == 0) TS(51);
    if (s == 9) TS(53);
    if (s == 19) TS(55);
  }
  STAMP(7);
  TS(57);
  wait_lgkm0();
  bar();
  TS(58);
#pragma unroll
  for (int lb = 1; lb < 4; lb++)
#pragma unroll
    for (int hh = 0; hh < 2; hh++) {
      const int r0 = (lb & 1) * 8 + hh * 4;
      *(LAS f32x4*)(smem + ((hf * 4 + ((c + lb) & 3)) * 3 + lb - 1) * 2048 + hh * 1024 + lane * 16) =
          (f32x4){o2[lb >> 1][r0], o2[lb >> 1][r0 + 1], o2[lb >> 1][r0 + 2], o2[lb >> 1][r0 + 3]};
    }
  f32x4 u0 = {o2[0][0] + (bf_lo(xs0.x) + bf_lo(bv0.x)), o2[0][1] + (bf_hi(xs0.x) + bf_hi(bv0.x)), o2[0][2] + (bf_lo(xs0.y) + bf_lo(bv0.y)), o2[0][3] + (bf_hi(xs0.y) + bf_hi(bv0.y))};
  f32x4 u1 = {o2[0][4] + (bf_lo(xs1.x) + bf_lo(bv1.x)), o2[0][5] + (bf_hi(xs1.x) + bf_hi(bv1.x)), o2[0][6] + (bf_lo(xs1.y) + bf_lo(bv1.y)), o2[0][7] + (bf_hi(xs1.y) + bf_hi(bv1.y))};
  wait_lgkm0();
  TS(59);
  bar();
  TS(60);
#pragma unroll
  for (int i = 0; i < 3; i++) { u0 += *(const LAS f32x4*)(smem + (wave * 3 + i) * 2048 + lane * 16); u1 += *(const LAS f32x4*)(smem + (wave * 3 + i) * 2048 + 1024 + lane * 16); }
  {
    auto s0 = __builtin_amdgcn_permlane32_swap(pk_bf16(u0[0], u0[1]), pk_bf16(u1[0], u1[1]), false, false);
    auto s1 = __builtin_amdgcn_permlane32_swap(pk_bf16(u0[2], u0[3]), pk_bf16(u1[2], u1[3]), false, false);
    *(GAS u32x4*)(P.out + orow * 1024 + (ct * 64 + c * 16 + (lane >> 5) * 8) * 2) = (u32x4){s0[0], s1[0], s0[1], s1[1]};
  }
  STAMP(8);
  TS(61);
  { u64 until = stamps[8] + 8 * (stamps[AMP] - stamps[0]); while (__builtin_amdgcn_s_memrealtime() < until) __builtin_amdgcn_s_sleep(2); }
  if (tid == 0) for (int i = 0; i < NSTAMP; i++) ((GAS u64*)(P.ws + WS_DIAG))[blockIdx.x * 16 + i] = stamps[i];
}

extern "C" void kernel_launch(void* const* d_in, const int* in_sizes, int n_in, void* d_out, int out_size, void* d_ws, size_t ws_size,
                              hipStream_t stream) {
  Params P;
  P.x = (const char*)d_in[0]; P.wqkv = (const char*)d_in[1]; P.wo = (const char*)d_in[2];
  P.w1 = (const char*)d_in[4]; P.b1 = (const char*)d_in[5]; P.w2 = (const char*)d_in[6]; P.b2 = (const char*)d_in[7];
  P.out = (char*)d_out; P.ws = (char*)d_ws;
  fused_block<<<256, 512, 0, stream>>>(P);
}
```

```cpp
#include <hip/hip_runtime.h>
#include <stdint.h>

typedef __attribute__((ext_vector_type(8))) __bf16 bf16x8;
typedef __attribute__((ext_vector_type(2))) __bf16 bf16x2;
typedef __attribute__((ext_vector_type(4))) short s16x4;
typedef __attribute__((ext_vector_type(4))) float f32x4;
typedef __attribute__((ext_vector_type(2))) float f32x2;
typedef __attribute__((ext_vector_type(16))) float f32x16;
typedef __attribute__((ext_vector_type(4))) unsigned u32x4;
typedef __attribute__((ext_vector_type(2))) unsigned u32x2;
#define LAS __attribute__((address_space(3)))
#define GAS __attribute__((address_space(1)))
typedef unsigned long long u64;

#define DIAG 0
#define PF 5
#define NE 4
#define BG 3
#define ROT1_MODE 0
#define XF 1
#define POLL 0
#define PRIO 1
#define DSIDE 1
#define CODEL 512
#define STE 0
#define WPF 1
#define SBC 0
#define CPF 1

constexpr int NK16 = 118;
constexpr int SLOT = 43008, XB = 18432;
constexpr int ST_MEAN = 129024, ST_CS = ST_MEAN + 1152, PAIRF = ST_MEAN + 1920, KVX = 19 * 8192;
constexpr int WS_FLAG = 1024, WS_DIAG = 32768, WS_STAT = 65536, WS_ACT = 131072, WS_TS = 16 * 1024 * 1024;
constexpr int ACT_BYTES = 64 * NK16 * 1024;
constexpr int NSTAMP = 9;
constexpr int CODE_LINES = CODEL;
constexpr unsigned SPIN_CAP = 1u << 20;
constexpr unsigned ONES = 0x3f803f80u;

struct Params {
  const char* x; const char* wqkv; const char* wo; const char* w1; const char* b1; const char* w2; const char* b2;
  char* out; char* ws;
};

#define SB() __builtin_amdgcn_sched_barrier(0)
__device__ __forceinline__ void set_m0(unsigned base, int add) { asm volatile("s_add_i32 m0, %0, %1" :: "s"(base), "i"(add) : "memory", "scc"); }
__device__ __forceinline__ void nop1() { asm volatile("s_nop 0"); }
template <int IMM> __device__ __forceinline__ void glds(unsigned voff, const char* sbase) {
  asm volatile("global_load_lds_dwordx4 %0, %1 offset:%2" :: "v"(voff), "s"(sbase), "i"(IMM) : "memory");
}
__device__ __forceinline__ void glds_mask(unsigned voff, const char* sbase, u64 mask) {
  asm volatile("s_mov_b64 exec, %2\n\tglobal_load_lds_dwordx4 %0, %1\n\ts_mov_b64 exec, -1" :: "v"(voff), "s"(sbase), "s"(mask) : "memory");
}
template <int N> __device__ __forceinline__ void wait_vm() { asm volatile("s_waitcnt vmcnt(%0) lgkmcnt(0)" :: "i"(N) : "memory"); }
__device__ __forceinline__ void wait_lgkm0() { asm volatile("s_waitcnt lgkmcnt(0)" ::: "memory"); }
__device__ __forceinline__ void bar() { asm volatile("s_barrier" ::: "memory"); }
__device__ __forceinline__ unsigned pk_bf16(float a, float b) { f32x2 p = {a, b}; return __builtin_bit_cast(unsigned, __builtin_convertvector(p, bf16x2)); }
__device__ __forceinline__ u32x2 trd(const LAS char* p) { return __builtin_bit_cast(u32x2, __builtin_amdgcn_ds_read_tr16_b64_v4i16((LAS s16x4*)p)); }
__device__ __forceinline__ bf16x8 b8(u32x4 v) { return __builtin_bit_cast(bf16x8, v); }
__device__ __forceinline__ float bf_lo(unsigned v) { return __uint_as_float(v << 16); }
__device__ __forceinline__ float bf_hi(unsigned v) { return __uint_as_float(v & 0xffff0000u); }
__device__ __forceinline__ float add_f32(float a, float b) { float r; asm("v_add_f32 %0, %1, %2" : "=v"(r) : "v"(a), "v"(b)); return r; }
__device__ __forceinline__ float sub_f32(float a, float b) { float r; asm("v_sub_f32 %0, %1, %2" : "=v"(r) : "v"(a), "v"(b)); return r; }
__device__ __forceinline__ float sel0(float v, u64 m) { float r; asm volatile("v_cndmask_b32 %0, %1, 0, %2" : "=v"(r) : "v"(v), "s"(m)); return r; }
__device__ __forceinline__ float max3f(float a, float b, float c) { return fmaxf(fmaxf(a, b), c); }
__device__ __forceinline__ u64 mix64(u64 z) {
  z += 0x9E3779B97F4A7C15ull; z = (z ^ (z >> 30)) * 0xBF58476D1CE4E5B9ull; z = (z ^ (z >> 27)) * 0x94D049BB133111EBull; return z ^ (z >> 31);
}
template <int CTRL> __device__ __forceinline__ float dpp_add(float v) {
  return v + __builtin_bit_cast(float, __builtin_amdgcn_update_dpp(0, __builtin_bit_cast(int, v), CTRL, 0xf, 0xf, true));
}
__device__ __forceinline__ float half_sum(float v) {
  v = dpp_add<0xB1>(v); v = dpp_add<0x4E>(v); v = dpp_add<0x141>(v); v = dpp_add<0x140>(v);
  auto a = __builtin_amdgcn_permlane16_swap(__float_as_uint(v), __float_as_uint(v), false, false);
  return __uint_as_float(a[0]) + __uint_as_float(a[1]);
}
#define LDSP(T, a) ((LAS T*)(size_t)(a))

#define STAMP(i)
#define TS(i)

__device__ __forceinline__ constexpr int b2grp(int s) {
  return s < 3 ? 16 + s : s < 7 ? 8 + s : s < 18 ? s - 7 : s < 21 ? s - 2 : s < 25 ? s - 10 : s < 28 ? s - 25 : s == 28 ? 15 : 19;
}
struct B2Ctx { const char* wo; const char* w2; int a0, rota, f0, nf, rotf; unsigned voff, vhalf, mhalf, mboth, vq[4], vr[4]; int tb0; };
__device__ __forceinline__ void b2_scalars(B2Ctx& b, const char* wo, const char* w2, unsigned ldsb, int hf, int c, int rt, int ct) {
  b.wo = wo + ct * 128; b.w2 = w2 + ct * 128; b.mboth = ldsb + c * 2048; b.mhalf = b.mboth + hf * 1024;
  b.a0 = 8 * c; b.f0 = c < 3 ? 22 * c : 65; b.nf = c < 2 ? 22 : 21;
  b.rota = ((ct + rt) & 1) * 4; b.rotf = ((ct + rt) & 1) * 10;
}
__device__ __forceinline__ int phys_k16(const B2Ctx& c, int s) {
  if (s < 8) return c.a0 + (s ^ c.rota);
  int t = s - 8 + c.rotf; t = t >= c.nf ? t - c.nf : t;
  return 32 + c.f0 + t;
}
__device__ __forceinline__ const char* b2_src(const B2Ctx& c, int s) {
  return s < 8 ? c.wo + phys_k16(c, s) * 16384 : c.w2 + (phys_k16(c, s) - 32) * 16384;
}
__device__ __forceinline__ void b2_issue(const B2Ctx& c, int s) {
  const char* src = b2_src(c, s);
  set_m0(c.mhalf, b2grp(s) * 8192); nop1();
  glds<0>(c.vhalf, src);
}
__device__ __forceinline__ void b2_issue2(const B2Ctx& c, int s) {
  const char* src = b2_src(c, s);
  set_m0(c.mboth, b2grp(s) * 8192); nop1();
  glds<0>(c.vq[0], src); glds<1024>(c.vr[0], src);
}
struct BRegs { u32x2 lo[10][2], hi[10][2]; };
__device__ __forceinline__ void br_read(BRegs& BR, const LAS char* smem, int tb0, int i) {
  const int s = i >> 2, cb = (i >> 1) & 1;
  if (i & 1) BR.hi[s][cb] = trd(smem + b2grp(s) * 8192 + (tb0 ^ (cb * 64)) + 512); else BR.lo[s][cb] = trd(smem + b2grp(s) * 8192 + (tb0 ^ (cb * 64)));
}

struct Ld { unsigned vo[6]; const char* base0; unsigned mbase; };
struct Epi { f32x4 mp, mc, mp1; unsigned selA, selB; float NEG; float mu[5], rs[5], csv; f32x4 mu4[5], rs4[5], cs[3]; u32x2 b1v[3]; };

template <bool H0, bool QKV, int NS>
struct P1 {
  static constexpr int NRB = H0 ? 5 : 4;
  static constexpr int NL = H0 ? (QKV ? 5 : 4) : 6;
  static constexpr int KSTRIDE = H0 ? 128 : QKV ? 64 * 3072 : 64 * 2752;
  struct Frag { u32x4 A[NRB]; u32x2 Bl[3], Bh[3]; };
  Ld ld; int rot, hdelta; unsigned mbaseh; u64 hmask;
  const char* cb;
  unsigned pb, pa0, pa1;
  u32x4 ones;

  __device__ __forceinline__ void open(int ch) { cb = ld.base0 + ((ch + rot) & 7) * KSTRIDE; set_m0(ld.mbase, (ch % 3) * SLOT); }
  template <int I> __device__ __forceinline__ void ld1(int ch) const {
    if (I >= NL) return;
    if (H0 && I == 4) { set_m0(mbaseh, (ch % 3) * SLOT); glds_mask(ld.vo[4], cb + hdelta, hmask); return; }
    glds<(H0 ? I * 1024 : I * 1024 - 3072)>(ld.vo[I], cb);
  }
  __device__ __forceinline__ void load1(int ch, int i) {
    if (i == 0) { open(ch); nop1(); ld1<0>(ch); } else if (i == 1) ld1<1>(ch); else if (i == 2) ld1<2>(ch);
    else if (i == 3) ld1<3>(ch); else if (i == 4) ld1<4>(ch); else ld1<5>(ch);
  }
  __device__ __forceinline__ void rd(Frag& f, int ks, int n) const {
    constexpr int ord[11] = {0, 1, 6, 2, 3, 4, 5, 7, 8, 9, 10};
    if (n >= 11) return;
    const int i = ord[n];
    if (i < 6) {
      const int s = i >> 1;
      if (s >= NS) return;
      if (i & 1) f.Bh[s] = trd(LDSP(const char, pb) + (32 * ks + 4) * 384 + s * 128); else f.Bl[s] = trd(LDSP(const char, pb) + (32 * ks) * 384 + s * 128);
    } else if (i - 6 < NRB) {
      const int r = H0 ? (i - 5) % 5 : i - 6;
      if (!(H0 && !QKV && r == 0)) f.A[r] = *(const LAS u32x4*)(LDSP(const char, ks ? pa1 : pa0) + r * 2048);
    }
  }
  __device__ __forceinline__ void advance(int delta) {
    asm volatile("v_add_u32 %0, %3, %0\n\tv_add_u32 %1, %3, %1\n\tv_add_u32 %2, %3, %2" : "+v"(pb), "+v"(pa0), "+v"(pa1) : "i"(delta));
  }
  template <bool LAST = false, class F> __device__ __forceinline__ void mma(f32x4 (&acc)[5][3], Frag& f, F&& fill) const {
#pragma unroll
    for (int rr = 0; rr < NRB; rr++) {
      const int r = !H0 ? rr : LAST ? (rr + 4) % 5 : (rr + 1) % 5;
#pragma unroll
      for (int s = 0; s < 3; s++) {
        if (s < NS) {
          const u32x4 B = {f.Bl[s].x, f.Bl[s].y, f.Bh[s].x, f.Bh[s].y};
          const u32x4 A = (H0 && !QKV && r == 0) ? ones : f.A[r];
          if (QKV && s == 2) acc[r][s] = __builtin_amdgcn_mfma_f32_16x16x32_bf16(b8(A), b8(B), acc[r][s], 0, 0, 0);
          else acc[r][s] = __builtin_amdgcn_mfma_f32_16x16x32_bf16(b8(B), b8(A), acc[r][s], 0, 0, 0);
          SB();
        }
        fill(3 * rr + s);
        SB();
      }
    }
  }
  template <class F> __device__ __forceinline__ void stepA(f32x4 (&acc)[5][3], Frag& cur, Frag& nx, int delta, F&& site) {
    mma(acc, cur, [&](int n) {
      rd(nx, 1, n);
      constexpr int S0 = H0 ? 4 : 2;
      if (n >= S0 && (n - S0) % 4 == 0 && (n - S0) / 4 < 3) site((n - S0) / 4);
      if (n == 3 * NRB - 2 && delta != 0) advance(delta);
    });
  }
  template <class S, class F> __device__ __forceinline__ void stepB(f32x4 (&acc)[5][3], Frag& cur, Frag& nx, S&& sync, F&& site) const {
    mma(acc, cur, [&](int n) {
      if (n == BG - 1) sync();
      if (n >= BG - 1) { const int i = n - (BG - 1); if (i < 2) { rd(nx, 0, 2 * i); rd(nx, 0, 2 * i + 1); } else rd(nx, 0, i + 2); }
      constexpr int S0 = H0 ? 6 : 5, SD = H0 ? 4 : 3;
      if (n >= S0 && (n - S0) % SD == 0 && (n - S0) / SD < 3) site((n - S0) / SD);
    });
  }
};

template <bool H0, bool QKV, int NSB>
__device__ __forceinline__ void phase1(const Params& P, LAS char* smem, unsigned ldsb, const Ld& ld, int p, int j, int c, int rot, int rt, int ct,
                                       B2Ctx& b2c, BRegs& BR, u64& tag, u64 (&stamps)[NSTAMP], unsigned& tsv) {
  typedef P1<H0, QKV, NSB> T;
  constexpr int NL = T::NL, HF = H0 ? 0 : 1;
  const int lane = threadIdx.x & 63;
  f32x4 acc[5][3]; Epi ep;
  T t;
  t.ld = ld; t.rot = rot;
  t.mbaseh = ldsb + 4 * c * 128; t.hdelta = p > 0 ? -16384 : 0; t.hmask = c == 0 ? 0xffff0000ull : 0xffffffffull;
  u64 e_old;
  {
    const int g = lane >> 4, m = lane & 15, q4 = m >> 2, pp = lane & 3;
    t.pa0 = ldsb + (H0 ? 0 : 5 * 2048) + m * 128 + ((g ^ (m >> 1)) * 16); t.pa1 = t.pa0 ^ 64;
    t.pb = ldsb + XB + (8 * g + q4) * 384 + (((2 * c + (pp >> 1)) ^ (2 * ((q4 >> 1) | ((g & 1) << 1)))) * 16) + (pp & 1) * 8;
    const unsigned o = m == 0 ? ONES : 0u; t.ones = (u32x4){o, o, o, o};
    if (H0 && !QKV) asm volatile("" : "+v"(t.ones));
    if (H0 && QKV) { if (c == 0 && lane < 48) *(LAS u32x4*)(smem + g * SLOT + m * 16) = (u32x4){ONES, ONES, ONES, ONES}; }
  }
#pragma unroll
  for (int r = 0; r < 5; r++)
#pragma unroll
    for (int s = 0; s < 3; s++) acc[r][s] = (f32x4){0.f, 0.f, 0.f, 0.f};
  typename T::Frag F0, F1;
  constexpr int NX = H0 ? 2 : 0, NSS = H0 ? 1 : 0;
  const int srow = p * 128 + ((QKV ? 0 : 8) + j) * 8 + 2 * c;
  u32x4 xr0, xr1; float xv[16], mean, sq; unsigned ssa;
  auto row_stats = [&](int k) {
    if (k == 0) {
#pragma unroll
      for (int e = 0; e < 4; e++) { xv[2 * e] = bf_lo(xr0[e]); xv[2 * e + 1] = bf_hi(xr0[e]); xv[8 + 2 * e] = bf_lo(xr1[e]); xv[9 + 2 * e] = bf_hi(xr1[e]); }
      float s0 = xv[0], s1 = xv[8];
#pragma unroll
      for (int e = 1; e < 8; e++) { s0 = add_f32(s0, xv[e]); s1 = add_f32(s1, xv[8 + e]); }
      mean = s0 + s1;
      asm volatile("" : "+v"(mean));
    }
    if (k == 1) { mean = half_sum(mean) * (1.f / 512.f); asm volatile("" : "+v"(mean)); }
    if (k == 2) {
      float q0 = 0.f, q1 = 0.f;
#pragma unroll
      for (int e = 0; e < 8; e++) { float d = sub_f32(xv[e], mean), d2 = sub_f32(xv[8 + e], mean); q0 = fmaf(d, d, q0); q1 = fmaf(d2, d2, q1); asm volatile("" : "+v"(q0), "+v"(q1)); }
      sq = q0 + q1;
      asm volatile("" : "+v"(sq));
    }
    if (k == 3) { sq = __builtin_amdgcn_rsqf(half_sum(sq) * (1.f / 512.f) + 1e-5f); asm volatile("" : "+v"(sq)); }
    if (k == 4) {
      const u32x2 gr = {__float_as_uint((lane & 1) ? sq : mean), (unsigned)tag};
      asm volatile("s_mov_b64 exec, %3\n\tglobal_store_dwordx2 %0, %1, %2 sc1\n\ts_mov_b64 exec, -1" :: "v"(ssa), "v"(gr), "s"(P.ws + WS_STAT), "s"(0x0000000300000003ull) : "memory");
    }
  };
  if (H0) {
    if (!XF) asm volatile("global_load_dwordx4 %0, %2, %3\n\tglobal_load_dwordx4 %1, %2, %3 offset:512" : "=&v"(xr0), "=&v"(xr1) : "v"((lane >> 5) * 1024 + (lane & 31) * 16), "s"(P.x + srow * 1024) : "memory");
    ssa = (srow + (lane >> 5)) * 16 + (lane & 1) * 8; asm volatile("" : "+v"(ssa));
  }
  asm volatile("s_load_dwordx2 %0, %1, 0x0" : "=s"(e_old) : "s"(P.ws) : "memory");
  unsigned pfd = 0, pfo; const char* pfw;
  auto warm = [&](int k) {
    asm volatile("s_mov_b64 exec, %3\n\tglobal_load_dword %0, %1, %2\n\ts_mov_b64 exec, -1" : "+v"(pfd) : "v"(pfo), "s"(pfw), "s"(0x3ull << (2 * k)) : "memory");
  };
  unsigned wpo; const char* wpw;
  constexpr int NW = (H0 && WPF) ? 1 : 0;
  auto w_warm = [&]() { asm volatile("global_load_dword %0, %1, %2" : "+v"(pfd) : "v"(wpo), "s"(wpw) : "memory"); };
  if (NW) {
    const int w0 = blockIdx.x * 91 + c * 23, wl = c == 3 ? 21 : 22, wn = 12288 - w0;
    wpw = w0 < 12288 ? P.wqkv + w0 * 128 : P.w1 + (w0 - 12288) * 128;
    const int wt = (w0 < 12288 && wn - 1 < wl) ? wn - 1 : wl;
    wpo = (lane < wt ? lane : wt) * 128; asm volatile("" : "+v"(wpo), "+s"(wpw));
    if (WPF == 3) w_warm();
  }
  unsigned sto, stw, str1, str4, csw, csw1, kvw, vq[4], vr[4];
  const char *bA, *bB, *bC;
  auto later = [&](int k) {
    const int g = lane >> 4, m = lane & 15, w = 4 * HF + c;
    if (k == 0) {
      u64 z = mix64(e_old); unsigned lo = __builtin_amdgcn_readfirstlane((unsigned)z), hi = __builtin_amdgcn_readfirstlane((unsigned)(z >> 32));
      asm volatile("" : "+s"(lo), "+s"(hi)); tag = ((u64)hi << 32) | lo;
    }
    if (k == 1) {
      const int l0 = blockIdx.x * 59 + w * 8, lim = blockIdx.x * 59 + 58 - l0;
      const int n0 = 4096 - l0;
      pfw = l0 < 4096 ? P.wo + l0 * 128 : P.w2 + (l0 - 4096) * 128;
      const int top = (l0 < 4096 && n0 - 1 < lim) ? n0 - 1 : lim;
      const int l = lane & 7;
      pfo = (l < top ? l : top) * 128; asm volatile("" : "+v"(pfo), "+s"(pfw));
      const int sti = 18 * w + (lane < 18 ? lane : 17), row = p * 128 - 16 + sti;
      sto = (row < 0 ? 0 : row) * 16; stw = ldsb + ST_MEAN + sti * 4; str1 = ldsb + ST_MEAN + (64 * HF + m) * 4;
      str4 = QKV ? ldsb + ST_MEAN + (64 * HF + 4 * g) * 4 : ldsb + KVX + (c * 16 + 4 * g) * 2;
      csw = ldsb + ST_CS + (c * 16 + 4 * g) * 4; csw1 = ldsb + ST_CS + (128 + c * 16 + m) * 4; kvw = ldsb + KVX + c * 2048 + lane * 16;
      asm volatile("" : "+v"(sto), "+v"(stw), "+v"(str1), "+v"(str4), "+v"(csw));
      if (QKV) asm volatile("" : "+v"(csw1), "+v"(kvw));
      b2c.voff = (lane >> 3) * 1024 + (((lane & 7) ^ ((g & 1) << 2)) * 16);
#pragma unroll
      for (int i = 0; i < 4; i++) vq[i] = b2c.voff + i * 16384;
      b2c.vhalf = b2c.voff + HF * 8192;
      b2c.tb0 = c * 2048 + (8 * (g >> 1) + (m >> 2)) * 128 + (((2 * (((g & 1) + c) & 3) + ((lane & 3) >> 1)) ^ ((m >> 3) << 2)) * 16) + (lane & 1) * 8;
      asm volatile("" : "+v"(b2c.vhalf), "+v"(b2c.tb0));
      if (H0) {
#pragma unroll
        for (int i = 0; i < 4; i++) { vr[i] = vq[i] + 7168; asm volatile("" : "+v"(vq[i]), "+v"(vr[i])); b2c.vq[i] = vq[i]; b2c.vr[i] = vr[i]; }
      }
      if (QKV) {
        constexpr u64 MP[4] = {0x555005500050000ull, 0xaaa00aa000a0000ull, 0x1555015500150001ull, 0x2aaa02aa002a0002ull}, MC[4] = {0x5000550055505555ull, 0xa000aa00aaa0aaaaull, 0x4000540055405554ull, 0x8000a800aa80aaa8ull};
        ep.NEG = -3.0e38f; asm volatile("" : "+v"(ep.NEG));
        const u64 live = (H0 && p == 0) ? 0ull : ~0ull;
#pragma unroll
        for (int e = 0; e < 4; e++) {
          ep.mp[e] = sel0(ep.NEG, MP[e]); ep.mc[e] = sel0(ep.NEG, MC[e]);
          ep.mp1[e] = H0 ? sel0(ep.NEG, MP[e] & live) : ep.mp[e];
        }
        ep.selA = (m & 1) ? 0x01000c0cu : 0x0c0c0100u; ep.selB = ep.selA + ((m & 1) ? 0x02020000u : 0x00000202u);
        asm volatile("" : "+v"(ep.selA), "+v"(ep.selB));
      }
    }
    if (k == 2 && H0) {
      b2_scalars(b2c, P.wo, P.w2, ldsb, HF, c, rt, ct);
      bA = b2_src(b2c, 0); asm volatile("" : "+s"(bA), "+s"(b2c.mboth));
    }
    if (k == 3 && H0) { bB = b2_src(b2c, 4); asm volatile("" : "+s"(bB)); }
    if (k == 4 && H0) { bC = b2_src(b2c, 8); asm volatile("" : "+s"(bC)); }
    if (k == 5 && H0) { if (lane == 0) *LDSP(unsigned, ldsb + PAIRF + c * 4) = 0u; }
  };
  auto b2_early = [&](int i) {
    if (H0) { const char* src = i < 4 ? bA : i < 8 ? bB : bC; set_m0(b2c.mboth, b2grp(i) * 8192); nop1(); glds<0>(vq[i & 3], src); glds<1024>(vr[i & 3], src); }
  };
  later(1);
  SB();
  TS(63);
  if (NW && WPF == 4) w_warm();
  constexpr int NWE = (WPF == 3 || WPF == 4) ? NW : 0;
  if (!(H0 && SBC)) {
#pragma unroll
    for (int i = 0; i < 6; i++) t.load1(1, i);
  }
  asm volatile("s_waitcnt lgkmcnt(0)" : "+s"(e_old) :: "memory");
  later(0);
  if (H0) {
    wait_vm<(SBC ? NL : 2 * NL) + NWE>();
    TS(2);
    { const LAS char* q = smem + 16 * 8192 + c * 2048 + (lane >> 5) * 1024 + (lane & 31) * 16; xr0 = *(const LAS u32x4*)q; xr1 = *(const LAS u32x4*)(q + 512); }
#pragma unroll
    for (int k = 0; k < 5; k++) row_stats(k);
    if (SBC) {
#pragma unroll
      for (int i = 0; i < 6; i++) t.load1(1, i);
    }
  }
  SB();
  TS(3);
  wait_vm<NL + NSS + NWE>();
  if (NW && WPF == 1) w_warm();
  TS(4);
  bar();
  TS(5);
  STAMP(1);
#pragma unroll
  for (int n = 0; n < 11; n++) t.rd(F0, 0, n);
  u32x4 st;
  t.stepA(acc, F0, F1, SLOT, [&](int k) { t.load1(2, 2 * k); t.load1(2, 2 * k + 1); if (NW && WPF == 2 && k == 2) w_warm(); });
  t.stepB(acc, F1, F0, [&]() { TS(6); wait_vm<NL + (SBC ? 0 : NSS) + NW - NWE>(); TS(7); bar(); TS(8); }, [&](int k) { t.load1(3, k); });
#pragma unroll
  for (int ch = 1; ch < 5; ch++) {
    t.stepA(acc, F0, F1, ch % 3 == 2 ? -2 * SLOT : SLOT, [&](int k) {
      if (H0 && !XF && ch == 2 && k == 0) row_stats(3);
      t.load1(ch + 2, 3 + k);
      if (k == 2) { warm(ch - 1); if (H0 && !XF && ch == 2) row_stats(4); }
    });
    t.stepB(acc, F1, F0, [&]() {
      TS(6 + 3 * ch);
      if (ch == 1) { if (H0 && !XF) asm volatile("s_waitcnt vmcnt(%2) lgkmcnt(0)" : "+v"(xr0), "+v"(xr1) : "i"(NL + 1 + (WPF == 2 ? NW : 0)) : "memory"); else wait_vm<NL + 1 + (WPF == 2 ? NW : 0)>(); }
      else if (ch < 4) wait_vm<NL + 2 + (XF ? 0 : NSS)>();
      else wait_vm<NL + 2>();
      TS(7 + 3 * ch); bar(); TS(8 + 3 * ch);
      if (STE && ch == 4) asm volatile("global_load_dwordx4 %0, %1, %2 sc1" : "=v"(st) : "v"(sto), "s"(P.ws + WS_STAT) : "memory");
    }, [&](int k) {
      t.load1(ch + 3, k);
      if (H0 && !XF && ch == 1) row_stats(k);
      if (k == 1) later(1 + ch);
    });
  }
  auto stats_to_lds = [&]() {
    const unsigned tg = (unsigned)tag;
    if (!__all(st.y == tg && st.w == tg)) {
      const GAS u64* sp = (const GAS u64*)(P.ws + WS_STAT + sto);
#pragma unroll 1
      for (unsigned spin = 0; spin < SPIN_CAP; spin++) {
        u64 a = __hip_atomic_load(sp, __ATOMIC_RELAXED, __HIP_MEMORY_SCOPE_AGENT), b = __hip_atomic_load(sp + 1, __ATOMIC_RELAXED, __HIP_MEMORY_SCOPE_AGENT);
        st = (u32x4){(unsigned)a, (unsigned)(a >> 32), (unsigned)b, (unsigned)(b >> 32)};
        if (__all(st.y == tg && st.w == tg)) break;
        __builtin_amdgcn_s_sleep(1);
      }
    }
    *LDSP(unsigned, stw) = st.x; *LDSP(unsigned, stw + 576) = st.z;
  };
  auto row_ops = [&](int r) {
    ep.mu[r] = *LDSP(const float, str1 + 64 * r); ep.rs[r] = *LDSP(const float, str1 + 64 * r + 576);
    if (QKV) { ep.mu4[r] = *LDSP(const f32x4, str4 + 64 * r); ep.rs4[r] = *LDSP(const f32x4, str4 + 64 * r + 576); }
    else if (r < 4) ep.b1v[r - 1] = *LDSP(const u32x2, str4 + 128 * (r - 1));
  };
  t.stepA(acc, F0, F1, -2 * SLOT, [&](int k) { t.load1(7, 3 + k); });
  t.stepB(acc, F1, F0, [&]() {
    TS(21); asm volatile("s_waitcnt vmcnt(%1) lgkmcnt(0)" : "+v"(pfd) : "i"(NL + STE) : "memory"); TS(22); bar(); TS(23);
    if (!STE) asm volatile("global_load_dwordx4 %0, %1, %2 sc1" : "=v"(st) : "v"(sto), "s"(P.ws + WS_STAT) : "memory");
  }, [&](int k) { b2_early(k); });
  t.stepA(acc, F0, F1, SLOT, [&](int k) {
    b2_early(3 + k);
    if (STE && k == 2) { asm volatile("s_waitcnt vmcnt(%1)" : "+v"(st) : "i"(NL + (H0 ? 12 : 0)) : "memory"); stats_to_lds(); }
  });
  t.stepB(acc, F1, F0, [&]() { TS(24); wait_vm<(H0 ? 12 : 0) + 1 - STE>(); TS(25); bar(); TS(26); }, [&](int k) { b2_early(6 + k); });
  t.stepA(acc, F0, F1, 0, [&](int k) { b2_early(9 + k); });
  TS(27);
  t.template mma<true>(acc, F1, [&](int n) {
    if (STE) { const int r = n + (QKV ? 0 : 1); if (r < 5) row_ops(r); }
    else if (n == 1) { asm volatile("s_waitcnt vmcnt(%1)" : "+v"(st) : "i"(H0 ? 24 : 0) : "memory"); stats_to_lds(); }
    if (H0 && QKV && n == 6) { *LDSP(f32x4, kvw) = acc[4][1]; *LDSP(f32x4, kvw + 1024) = acc[4][2]; }
    if (H0 && n == 9) {
      if (QKV) { if (lane < 16) *LDSP(float, csw1) = acc[0][2][0]; }
      if ((lane & 15) == 0) {
        *LDSP(f32x4, csw) = acc[0][0];
        *LDSP(f32x4, csw + 256) = acc[0][1];
        if (!QKV) *LDSP(f32x4, csw + 512) = acc[0][2];
      }
    }
  });
  STAMP(2);
  if (!H0) {
#pragma unroll
    for (int r = 4; r >= 1; r--)
#pragma unroll
      for (int s = 0; s < 3; s++) acc[r][s] = acc[r - 1][s];
  }
  TS(29);
  wait_vm<H0 ? 24 - 2 * NE : 0>();
  if (PRIO == 1) { if (H0) __builtin_amdgcn_s_setprio(2); else __builtin_amdgcn_s_setprio(1); }
  if (PRIO == 2) { if (!H0) __builtin_amdgcn_s_setprio(1); }
  bar();
  TS(31);
  ep.cs[0] = *LDSP(const f32x4, csw); ep.cs[1] = *LDSP(const f32x4, csw + 256);
  if (QKV) ep.csv = *LDSP(const float, csw1); else ep.cs[2] = *LDSP(const f32x4, csw + 512);
  if (!H0 && QKV) { acc[0][1] = *LDSP(const f32x4, kvw); acc[0][2] = *LDSP(const f32x4, kvw + 1024); }
  if (!STE) {
#pragma unroll
    for (int r = QKV ? 0 : 1; r < 5; r++) row_ops(r);
  }
  SB();
  STAMP(3);
  constexpr int hf = HF; constexpr bool qkv = QKV, blk2 = NSB == 3;
  const __amdgpu_buffer_rsrc_t act = __builtin_amdgcn_make_buffer_rsrc(P.ws + WS_ACT, 0, ACT_BYTES, 0x00020000);
  const int rt32 = p * 4 + hf * 2, tb0 = b2c.tb0;
  const char* bD = bC + 65536;
  auto side = [&](int u) {
    SB();
    if (hf == 0) {
      if (!DSIDE) return;
      const char* src = u < 4 ? bD : bD + 65536;
      set_m0(b2c.mboth, b2grp(12 + u) * 8192); nop1(); glds<0>(b2c.vq[u & 3], src); glds<1024>(b2c.vr[u & 3], src);
    } else {
      if (u < 4) {
#pragma unroll
        for (int i = 0; i < NE; i++) br_read(BR, smem, tb0, NE * u + i);
      }
      if (u == 5) { wait_lgkm0(); if (lane == 0) *(LAS unsigned*)(smem + PAIRF + c * 4) = 1u; }
    }
    SB();
  };
  if (qkv) {
    u32x2 qf[5], kf[5], vf[5];
#pragma unroll
    for (int r = 0; r < 5; r++) {
      f32x4 k4 = (acc[r][1] - ep.mu[r] * ep.cs[1]) * ep.rs[r], v4 = (acc[r][2] - ep.mu4[r] * ep.csv) * ep.rs4[r];
      kf[r] = (u32x2){pk_bf16(k4[0], k4[1]), pk_bf16(k4[2], k4[3])};
      vf[r] = (u32x2){pk_bf16(v4[0], v4[1]), pk_bf16(v4[2], v4[3])};
      if (r > 0) { f32x4 q4v = (acc[r][0] - ep.mu[r] * ep.cs[0]) * ep.rs[r]; qf[r] = (u32x2){pk_bf16(q4v[0], q4v[1]), pk_bf16(q4v[2], q4v[3])}; }
      side(r);
    }
    TS(32);
    const float SC = 0.25f * 1.4426950408889634f, NEG = ep.NEG;
    float sv[4][4], mx[4];
#pragma unroll
    for (int r = 1; r < 5; r++) {
      f32x4 sp = __builtin_amdgcn_mfma_f32_16x16x16bf16_1k(__builtin_bit_cast(s16x4, kf[r - 1]), __builtin_bit_cast(s16x4, qf[r]), r == 1 ? ep.mp1 : ep.mp, 0, 0, 0);
      f32x4 sc = __builtin_amdgcn_mfma_f32_16x16x16bf16_1k(__builtin_bit_cast(s16x4, kf[r]), __builtin_bit_cast(s16x4, qf[r]), ep.mc, 0, 0, 0);
      sv[r - 1][0] = max3f(sp[0], sp[1], NEG); sv[r - 1][1] = max3f(sp[2], sp[3], NEG); sv[r - 1][2] = max3f(sc[0], sc[1], NEG); sv[r - 1][3] = max3f(sc[2], sc[3], NEG);
      mx[r - 1] = max3f(max3f(sv[r - 1][0], sv[r - 1][1], sv[r - 1][2]), sv[r - 1][3], NEG);
    }
    side(5);
    TS(36);
    {
      auto a = __builtin_amdgcn_permlane16_swap(__float_as_uint(mx[0]), __float_as_uint(mx[1]), false, false);
      auto b = __builtin_amdgcn_permlane16_swap(__float_as_uint(mx[2]), __float_as_uint(mx[3]), false, false);
      const float u = max3f(__uint_as_float(a[0]), __uint_as_float(a[1]), NEG), v = max3f(__uint_as_float(b[0]), __uint_as_float(b[1]), NEG);
      auto d = __builtin_amdgcn_permlane32_swap(__float_as_uint(u), __float_as_uint(v), false, false);
      const unsigned t = __float_as_uint(max3f(__uint_as_float(d[0]), __uint_as_float(d[1]), NEG) * SC);
      auto e = __builtin_amdgcn_permlane32_swap(t, t, false, false);
      auto f0 = __builtin_amdgcn_permlane16_swap(e[0], e[0], false, false);
      auto f1 = __builtin_amdgcn_permlane16_swap(e[1], e[1], false, false);
      mx[0] = __uint_as_float(f0[0]); mx[1] = __uint_as_float(f0[1]); mx[2] = __uint_as_float(f1[0]); mx[3] = __uint_as_float(f1[1]);
    }
    const u32x4 one4 = {ONES, ONES, ONES, ONES};
    const f32x4 z = {0.f, 0.f, 0.f, 0.f};
    f32x4 o4[4], l4[4];
#pragma unroll
    for (int r = 0; r < 4; r++) {
      float e[4];
#pragma unroll
      for (int i = 0; i < 4; i++) e[i] = __builtin_amdgcn_exp2f(sv[r][i] * SC - mx[r]);
      const unsigned pa = pk_bf16(e[0], e[1]), pb = pk_bf16(e[2], e[3]);
      const u32x4 pf = {__builtin_amdgcn_perm(0u, pa, ep.selA), __builtin_amdgcn_perm(0u, pa, ep.selB), __builtin_amdgcn_perm(0u, pb, ep.selA), __builtin_amdgcn_perm(0u, pb, ep.selB)};
      const u32x4 vv = {vf[r].x, vf[r].y, vf[r + 1].x, vf[r + 1].y};
      o4[r] = __builtin_amdgcn_mfma_f32_16x16x32_bf16(b8(vv), b8(pf), z, 0, 0, 0);
      l4[r] = __builtin_amdgcn_mfma_f32_16x16x32_bf16(b8(one4), b8(pf), z, 0, 0, 0);
    }
    u32x2 of[4];
#pragma unroll
    for (int r = 0; r < 4; r++) {
      const f32x4 o = o4[r] * __builtin_amdgcn_rcpf(l4[r][0]);
      of[r] = (u32x2){pk_bf16(o[0], o[1]), pk_bf16(o[2], o[3])};
    }
#pragma unroll
    for (int pr = 0; pr < 2; pr++) {
      auto s0 = __builtin_amdgcn_permlane16_swap(of[2 * pr].x, of[1 + 2 * pr].x, false, false);
      auto s1 = __builtin_amdgcn_permlane16_swap(of[2 * pr].y, of[1 + 2 * pr].y, false, false);
      __builtin_amdgcn_raw_buffer_store_b128((u32x4){s0[0], s1[0], s0[1], s1[1]}, act, lane * 16 + ((rt32 + pr) * NK16 + 4 * j + c) * 1024, 0, 16);
    }
    SB();
  } else {
    const unsigned sto0 = lane * 16 + (rt32 * NK16 + 32 + 11 * j + c) * 1024;
    auto gelu_block = [&](int s) {
      const f32x4 bb = {bf_lo(ep.b1v[s].x), bf_hi(ep.b1v[s].x), bf_lo(ep.b1v[s].y), bf_hi(ep.b1v[s].y)};
      f32x4 v[4], z[4];
#pragma unroll
      for (int r = 0; r < 4; r++) v[r] = acc[r + 1][s] - ep.mu[r + 1] * ep.cs[s];
      SB();
#pragma unroll
      for (int r = 0; r < 4; r++) v[r] = v[r] * ep.rs[r + 1] + bb;
      SB();
#pragma unroll
      for (int r = 0; r < 4; r++) z[r] = v[r] * v[r];
      if (s < 2) side(3 * s); else SB();
#pragma unroll
      for (int r = 0; r < 4; r++) z[r] = z[r] * (-0.10294324f) + (-2.3022082f);
      SB();
#pragma unroll
      for (int r = 0; r < 4; r++) z[r] = z[r] * v[r];
      SB();
#pragma unroll
      for (int r = 0; r < 4; r++) z[r] = (f32x4){__builtin_amdgcn_exp2f(z[r][0]), __builtin_amdgcn_exp2f(z[r][1]), __builtin_amdgcn_exp2f(z[r][2]), __builtin_amdgcn_exp2f(z[r][3])};
      if (s < 2) side(3 * s + 1); else SB();
#pragma unroll
      for (int r = 0; r < 4; r++) z[r] = z[r] + 1.f;
      SB();
#pragma unroll
      for (int r = 0; r < 4; r++) z[r] = (f32x4){__builtin_amdgcn_rcpf(z[r][0]), __builtin_amdgcn_rcpf(z[r][1]), __builtin_amdgcn_rcpf(z[r][2]), __builtin_amdgcn_rcpf(z[r][3])};
      if (s < 2) side(3 * s + 2); else SB();
#pragma unroll
      for (int r = 0; r < 4; r++) v[r] = v[r] * z[r];
      SB();
      u32x2 hfr[4];
#pragma unroll
      for (int r = 0; r < 4; r++) hfr[r] = (u32x2){pk_bf16(v[r][0], v[r][1]), pk_bf16(v[r][2], v[r][3])};
      SB();
#pragma unroll
      for (int pr = 0; pr < 2; pr++) {
        auto s0 = __builtin_amdgcn_permlane16_swap(hfr[2 * pr].x, hfr[2 * pr + 1].x, false, false);
        auto s1 = __builtin_amdgcn_permlane16_swap(hfr[2 * pr].y, hfr[2 * pr + 1].y, false, false);
        __builtin_amdgcn_raw_buffer_store_b128((u32x4){s0[0], s1[0], s0[1], s1[1]}, act, sto0 + (pr * NK16 + 4 * s) * 1024, 0, 16);
      }
      SB();
      TS(32 + s);
    };
    gelu_block(0);
    gelu_block(1);
    if (blk2) gelu_block(2);
  }
  STAMP(4);
  TS(40);
  if (hf == 0) {
    __builtin_amdgcn_s_setprio(0);
    if (!DSIDE) {
#pragma unroll
      for (int u = 0; u < 6; u++) { const char* src = u < 4 ? bD : bD + 65536; set_m0(b2c.mboth, b2grp(12 + u) * 8192); nop1(); glds<0>(b2c.vq[u & 3], src); glds<1024>(b2c.vr[u & 3], src); }
    }
#pragma unroll
    for (int i = 0; i < 4 * NE; i++) br_read(BR, smem, tb0, i);
#pragma unroll 1
    for (unsigned spin = 0; spin < SPIN_CAP; spin++)
      if (__builtin_amdgcn_readfirstlane(__hip_atomic_load((LAS unsigned*)(smem + PAIRF + c * 4), __ATOMIC_RELAXED, __HIP_MEMORY_SCOPE_WORKGROUP)) == 1u) break;
    wait_lgkm0();
#pragma unroll
    for (int s = 18; s < 18 + NE; s++) b2_issue2(b2c, s);
    TS(44);
    wait_vm<2 * NE + (DSIDE ? 0 : 12)>();
  } else {
    wait_vm<0>();
    __builtin_amdgcn_s_setprio(0);
  }
}

typedef __attribute__((ext_vector_type(16))) unsigned u32x16;
__device__ __forceinline__ char* mkptr(unsigned lo, unsigned hi) { return (char*)(((u64)hi << 32) | lo); }
__global__ __launch_bounds__(512, 2) void fused_block(Params) {
  u64 stamps[NSTAMP];
  unsigned tsv = 0;
  TS(0);
  STAMP(0);
  u32x16 ka; u32x2 kb;
  asm volatile("s_load_dwordx16 %0, %2, 0x0\n\ts_load_dwordx2 %1, %2, 0x40" : "=&s"(ka), "=&s"(kb) : "s"(__builtin_amdgcn_kernarg_segment_ptr()));
  Params P;
  auto args = [&]() {
    asm volatile("s_waitcnt lgkmcnt(0)" : "+s"(ka), "+s"(kb));
    P.x = mkptr(ka[0], ka[1]); P.wqkv = mkptr(ka[2], ka[3]); P.wo = mkptr(ka[4], ka[5]); P.w1 = mkptr(ka[6], ka[7]); P.b1 = mkptr(ka[8], ka[9]);
    P.w2 = mkptr(ka[10], ka[11]); P.b2 = mkptr(ka[12], ka[13]); P.out = mkptr(ka[14], ka[15]); P.ws = mkptr(kb[0], kb[1]);
  };
  __shared__ __attribute__((aligned(16))) char smem_[163840];
  LAS char* smem = (LAS char*)smem_;
  const unsigned ldsb = (unsigned)(size_t)smem;
  const int tid = threadIdx.x, lane = tid & 63, wave = __builtin_amdgcn_readfirstlane(tid >> 6);
  const int b = blockIdx.x, xcd = b & 7, r5 = b >> 3;
  unsigned cpd = 0;
  if (CPF == 1) {
    u64 pc; asm volatile("s_getpc_b64 %0" : "=s"(pc)); pc &= ~255ull;
    asm volatile("global_load_dword %0, %1, %2" : "+v"(cpd) : "v"(((tid < CODE_LINES / 32 ? tid : 0) * 32 + r5) * 128), "s"(pc) : "memory");
  }
  const int hf = wave >> 2, c = wave & 3, jj = r5 >> 3;
  const bool qkv = jj < 2;
  const int p = (xcd & 1) * 8 + (r5 & 7), j = (xcd >> 1) * 2 + (jj & 1);
  const int rot1 = ROT1_MODE == 0 ? 0 : ROT1_MODE == 1 ? (p + jj) : ROT1_MODE == 2 ? ((p + jj) & 1) * 4 : ((p + jj) & 3) * 2;
  const int rt = xcd * 4 + jj, ct = r5 & 7;
  Ld ld;
  const int pitch = qkv ? 3072 : 2752, k0 = rot1 & 7;
  int so0, ko0;
  if (hf == 0) {
    so0 = p * 131072 - 4096; ko0 = k0 * 128; ld.mbase = ldsb + (16 + 32 * c) * 128;
    asm volatile("" : "+s"(so0), "+s"(ko0), "+s"(ld.mbase));
    const unsigned v0 = 4096 + (32 * c + (lane >> 3)) * 1024 + (((lane & 7) ^ (lane >> 4)) * 16);
    ld.vo[0] = v0; ld.vo[1] = (v0 ^ 64) + 7168; ld.vo[2] = v0 + 14336; ld.vo[3] = (v0 ^ 64) + 21504;
    { const int row = 4 * c + ((lane >> 3) & 3); ld.vo[4] = 4096 + row * 1024 + (((lane & 7) ^ (row >> 1)) * 16); }
    ld.vo[5] = (lane < (j == 7 ? 18 : 22) ? lane : 0) * 16;
  } else {
    const int secst = qkv ? 1024 : 128, lim = qkv ? 23 : (j == 7 ? 17 : 21);
    so0 = j * (qkv ? 128 : 352) + c * 16 * pitch - 4096; ko0 = k0 * 64 * pitch; ld.mbase = ldsb + XB + c * 6144 + 3072;
    asm volatile("" : "+s"(so0), "+s"(ko0), "+s"(ld.mbase));
#pragma unroll
    for (int r = 0; r < 3; r++) {
      const int v = lane + 64 * r, row = (v * 171) >> 12, cc = (v - 24 * row) ^ (row & 2);
      const int c0 = cc < lim ? cc : lim, c1 = (cc ^ 4) < lim ? (cc ^ 4) : lim;
      ld.vo[r] = 4096 + 3072 - 1024 * r + row * pitch + (c0 >> 3) * secst + (c0 & 7) * 16;
      ld.vo[r + 3] = 4096 - 1024 * r + (row + 8) * pitch + (c1 >> 3) * secst + (c1 & 7) * 16;
    }
  }
  unsigned xvo = lane * 16;
  asm volatile("" : "+v"(ld.vo[0]), "+v"(ld.vo[1]), "+v"(ld.vo[2]), "+v"(ld.vo[3]), "+v"(ld.vo[4]), "+v"(ld.vo[5]), "+v"(xvo));
  SB();
  args();
  ld.base0 = (hf == 0 ? P.x : qkv ? P.wqkv : P.w1) + so0;
  TS(62);
  if (hf == 0) {
    const u64 hmask = c == 0 ? 0xffff0000ull : 0xffffffffull;
    if (XF) {
      const char* xrow = P.x + (p * 128 + ((qkv ? 0 : 8) + j) * 8 + 2 * c) * 1024;
      set_m0(ldsb + c * 2048, 16 * 8192); nop1(); glds<0>(xvo, xrow); glds<1024>(xvo, xrow);
    }
    const char* cb = ld.base0 + ko0;
    set_m0(ld.mbase, 0); nop1();
    glds<0>(ld.vo[0], cb); glds<1024>(ld.vo[1], cb); glds<2048>(ld.vo[2], cb); glds<3072>(ld.vo[3], cb);
    if (qkv) { set_m0(ldsb + 4 * c * 128, 0); glds_mask(ld.vo[4], cb + (p > 0 ? -16384 : 0), hmask); }
    if (!qkv && c == 0) { set_m0(ldsb, KVX); nop1(); glds<0>(ld.vo[5], P.b1 + j * 352); }
  } else {
    const char* cb = ld.base0 + ko0;
    set_m0(ld.mbase, 0); nop1();
    glds<-3072>(ld.vo[0], cb); glds<-2048>(ld.vo[1], cb); glds<-1024>(ld.vo[2], cb); glds<0>(ld.vo[3], cb); glds<1024>(ld.vo[4], cb); glds<2048>(ld.vo[5], cb);
  }
  SB();
  TS(1);
  B2Ctx b2c; BRegs BR; u64 tag;
#define P1ARGS P, smem, ldsb, ld, p, j, c, rot1, rt, ct, b2c, BR, tag, stamps, tsv
  const bool blk2 = 8 + c < ((j == 7) ? 9 : 11);
  if (qkv) { if (hf == 0) phase1<true, true, 3>(P1ARGS); else phase1<false, true, 3>(P1ARGS); }
  else if (blk2) { if (hf == 0) phase1<true, false, 3>(P1ARGS); else phase1<false, false, 3>(P1ARGS); }
  else { if (hf == 0) phase1<true, false, 2>(P1ARGS); else phase1<false, false, 2>(P1ARGS); }
  asm volatile("" :: "v"(cpd));
  b2_scalars(b2c, P.wo, P.w2, ldsb, hf, c, rt, ct);
  const __amdgpu_buffer_rsrc_t act = __builtin_amdgcn_make_buffer_rsrc(P.ws + WS_ACT, 0, ACT_BYTES, 0x00020000);
  const int tb0 = b2c.tb0;
  TS(41);
  bar();
  if (tid == 0) __hip_atomic_store((GAS u64*)(P.ws + WS_FLAG + p * 128 + (qkv ? j : 8 + j) * 8), tag, __ATOMIC_RELAXED, __HIP_MEMORY_SCOPE_AGENT);
  STAMP(5);
  TS(42);
#pragma unroll
  for (int i = 4 * NE; i < 40; i++) br_read(BR, smem, tb0, i);
  wait_lgkm0();
  bar();
#pragma unroll
  for (int s = 18 + NE; s < 30; s++) b2_issue(b2c, s);
  const int orow = rt * 64 + hf * 32 + (lane & 31), ocol = ct * 64 + c * 16 + (lane >> 5) * 4;
  const u32x2 xs0 = *(const GAS u32x2*)(P.x + orow * 1024 + ocol * 2), xs1 = *(const GAS u32x2*)(P.x + orow * 1024 + ocol * 2 + 16);
  const u32x2 bv0 = *(const GAS u32x2*)(P.b2 + ocol * 2), bv1 = *(const GAS u32x2*)(P.b2 + ocol * 2 + 16);
  TS(45);
  if (wave == 0) {
    if (b == 0) {
      const GAS u64* fa = (const GAS u64*)(P.ws + WS_FLAG) + lane;
#pragma unroll 1
      for (unsigned spin = 0; spin < SPIN_CAP; spin++) {
        u64 v0 = __hip_atomic_load(fa, __ATOMIC_RELAXED, __HIP_MEMORY_SCOPE_AGENT), v1 = __hip_atomic_load(fa + 64, __ATOMIC_RELAXED, __HIP_MEMORY_SCOPE_AGENT);
        u64 v2 = __hip_atomic_load(fa + 128, __ATOMIC_RELAXED, __HIP_MEMORY_SCOPE_AGENT), v3 = __hip_atomic_load(fa + 192, __ATOMIC_RELAXED, __HIP_MEMORY_SCOPE_AGENT);
        if (__all(v0 == tag && v1 == tag && v2 == tag && v3 == tag)) break;
        __builtin_amdgcn_s_sleep(1);
      }
      if (lane == 0) __hip_atomic_store((GAS u64*)P.ws, tag, __ATOMIC_RELAXED, __HIP_MEMORY_SCOPE_AGENT);
    } else {
      const GAS u64* fl = (const GAS u64*)(P.ws + WS_FLAG + (rt >> 1) * 128 + (lane & 15) * 8);
      if (POLL == 2) {
        u64 v0 = __hip_atomic_load(fl, __ATOMIC_RELAXED, __HIP_MEMORY_SCOPE_AGENT);
#pragma unroll 1
        for (unsigned spin = 0; spin < SPIN_CAP; spin++) {
          __builtin_amdgcn_s_sleep(2);
          u64 v1 = __hip_atomic_load(fl, __ATOMIC_RELAXED, __HIP_MEMORY_SCOPE_AGENT);
          if (__all(v0 == tag)) break;
          __builtin_amdgcn_s_sleep(2);
          v0 = __hip_atomic_load(fl, __ATOMIC_RELAXED, __HIP_MEMORY_SCOPE_AGENT);
          if (__all(v1 == tag)) break;
        }
      } else {
#pragma unroll 1
        for (unsigned spin = 0; spin < SPIN_CAP; spin++) {
          if (__all(__hip_atomic_load(fl, __ATOMIC_RELAXED, __HIP_MEMORY_SCOPE_AGENT) == tag)) break;
          if (POLL == 0) __builtin_amdgcn_s_sleep(1);
        }
      }
    }
  }
  TS(49);
  bar();
  TS(50);
  STAMP(6);
  const int abase = (rt * 2 + hf) * NK16;
  auto aload = [&](int s) -> u32x4 {
    const int vo = (s == 29 && b2c.nf == 21) ? 0x7f000000 : lane * 16;
    return __builtin_amdgcn_raw_buffer_load_b128(act, vo, (abase + phys_k16(b2c, s)) * 1024, 16);
  };
  u32x4 AF[PF];
  f32x16 o2[2];
#pragma unroll
  for (int e = 0; e < 16; e++) { o2[0][e] = 0.f; o2[1][e] = 0.f; }
#pragma unroll
  for (int s = 0; s < PF; s++) AF[s] = aload(s);
#pragma unroll
  for (int s = 0; s < 30; s++) {
    if (s == 12 && !DSIDE) { wait_lgkm0(); bar(); }
    if (s == 18) { wait_lgkm0(); bar(); }
    u32x4 B0, B1;
    if (s < 10) { B0 = (u32x4){BR.lo[s][0].x, BR.lo[s][0].y, BR.hi[s][0].x, BR.hi[s][0].y}; B1 = (u32x4){BR.lo[s][1].x, BR.lo[s][1].y, BR.hi[s][1].x, BR.hi[s][1].y}; }
    else {
      u32x2 lo = trd(smem + b2grp(s) * 8192 + tb0), hi = trd(smem + b2grp(s) * 8192 + tb0 + 512);
      B0 = (u32x4){lo.x, lo.y, hi.x, hi.y};
      lo = trd(smem + b2grp(s) * 8192 + (tb0 ^ 64)); hi = trd(smem + b2grp(s) * 8192 + (tb0 ^ 64) + 512);
      B1 = (u32x4){lo.x, lo.y, hi.x, hi.y};
    }
    const u32x4 a = AF[s % PF];
    o2[0] = __builtin_amdgcn_mfma_f32_32x32x16_bf16(b8(B0), b8(a), o2[0], 0, 0, 0);
    o2[1] = __builtin_amdgcn_mfma_f32_32x32x16_bf16(b8(B1), b8(a), o2[1], 0, 0, 0);
    if (s + PF < 30) AF[s % PF] = aload(s + PF);
    SB();
    if (s == 0) TS(51);
    if (s == 9) TS(53);
    if (s == 19) TS(55);
  }
  STAMP(7);
  TS(57);
  constexpr int XCH = 3 * 8192;
#pragma unroll
  for (int lb = 1; lb < 4; lb++)
#pragma unroll
    for (int hh = 0; hh < 2; hh++) {
      const int r0 = (lb & 1) * 8 + hh * 4;
      *(LAS f32x4*)(smem + XCH + ((hf * 4 + ((c + lb) & 3)) * 3 + lb - 1) * 2048 + hh * 1024 + lane * 16) =
          (f32x4){o2[lb >> 1][r0], o2[lb >> 1][r0 + 1], o2[lb >> 1][r0 + 2], o2[lb >> 1][r0 + 3]};
    }
  f32x4 u0 = {o2[0][0] + (bf_lo(xs0.x) + bf_lo(bv0.x)), o2[0][1] + (bf_hi(xs0.x) + bf_hi(bv0.x)), o2[0][2] + (bf_lo(xs0.y) + bf_lo(bv0.y)), o2[0][3] + (bf_hi(xs0.y) + bf_hi(bv0.y))};
  f32x4 u1 = {o2[0][4] + (bf_lo(xs1.x) + bf_lo(bv1.x)), o2[0][5] + (bf_hi(xs1.x) + bf_hi(bv1.x)), o2[0][6] + (bf_lo(xs1.y) + bf_lo(bv1.y)), o2[0][7] + (bf_hi(xs1.y) + bf_hi(bv1.y))};
  wait_lgkm0();
  TS(59);
  bar();
  TS(60);
#pragma unroll
  for (int i = 0; i < 3; i++) { u0 += *(const LAS f32x4*)(smem + XCH + (wave * 3 + i) * 2048 + lane * 16); u1 += *(const LAS f32x4*)(smem + XCH + (wave * 3 + i) * 2048 + 1024 + lane * 16); }
  {
    auto s0 = __builtin_amdgcn_permlane32_swap(pk_bf16(u0[0], u0[1]), pk_bf16(u1[0], u1[1]), false, false);
    auto s1 = __builtin_amdgcn_permlane32_swap(pk_bf16(u0[2], u0[3]), pk_bf16(u1[2], u1[3]), false, false);
    *(GAS u32x4*)(P.out + orow * 1024 + (ct * 64 + c * 16 + (lane >> 5) * 8) * 2) = (u32x4){s0[0], s1[0], s0[1], s1[1]};
  }
  STAMP(8);
  TS(61);
}

extern "C" void kernel_launch(void* const* d_in, const int* in_sizes, int n_in, void* d_out, int out_size, void* d_ws, size_t ws_size,
                              hipStream_t stream) {
  Params P;
  P.x = (const char*)d_in[0]; P.wqkv = (const char*)d_in[1]; P.wo = (const char*)d_in[2];
  P.w1 = (const char*)d_in[4]; P.b1 = (const char*)d_in[5]; P.w2 = (const char*)d_in[6]; P.b2 = (const char*)d_in[7];
  P.out = (char*)d_out; P.ws = (char*)d_ws;
  fused_block<<<256, 512, 0, stream>>>(P);
}
```

```cpp
#include <hip/hip_runtime.h>
#include <stdint.h>

typedef __attribute__((ext_vector_type(8))) __bf16 bf16x8;
typedef __attribute__((ext_vector_type(2))) __bf16 bf16x2;
typedef __attribute__((ext_vector_type(4))) short s16x4;
typedef __attribute__((ext_vector_type(4))) float f32x4;
typedef __attribute__((ext_vector_type(2))) float f32x2;
typedef __attribute__((ext_vector_type(16))) float f32x16;
typedef __attribute__((ext_vector_type(4))) unsigned u32x4;
typedef __attribute__((ext_vector_type(2))) unsigned u32x2;
#define LAS __attribute__((address_space(3)))
#define GAS __attribute__((address_space(1)))
typedef unsigned long long u64;

#define DIAG 0
#define PF 5
#define NE 4
#define BG 3
#define ROT1_MODE 0
#define XF 1
#define POLL 0
#define PRIO 1
#define DSIDE 1
#define CODEL 512
#define STE 0
#define WPF 1
#define SBC 0
#define OUTSC 0
#define P2PRIO 0
#define CPF 1

constexpr int NK16 = 118;
constexpr int SLOT = 43008, XB = 18432;
constexpr int ST_MEAN = 129024, ST_CS = ST_MEAN + 1152, PAIRF = ST_MEAN + 1920, KVX = 19 * 8192;
constexpr int WS_FLAG = 1024, WS_DIAG = 32768, WS_STAT = 65536, WS_ACT = 131072, WS_TS = 16 * 1024 * 1024;
constexpr int ACT_BYTES = 64 * NK16 * 1024;
constexpr int NSTAMP = 9;
constexpr int CODE_LINES = CODEL;
constexpr unsigned SPIN_CAP = 1u << 20;
constexpr unsigned ONES = 0x3f803f80u;

struct Params {
  const char* x; const char* wqkv; const char* wo; const char* w1; const char* b1; const char* w2; const char* b2;
  char* out; char* ws;
};

#define SB() __builtin_amdgcn_sched_barrier(0)
__device__ __forceinline__ void set_m0(unsigned base, int add) { asm volatile("s_add_i32 m0, %0, %1" :: "s"(base), "i"(add) : "memory", "scc"); }
__device__ __forceinline__ void nop1() { asm volatile("s_nop 0"); }
template <int IMM> __device__ __forceinline__ void glds(unsigned voff, const char* sbase) {
  asm volatile("global_load_lds_dwordx4 %0, %1 offset:%2" :: "v"(voff), "s"(sbase), "i"(IMM) : "memory");
}
__device__ __forceinline__ void glds_mask(unsigned voff, const char* sbase, u64 mask) {
  asm volatile("s_mov_b64 exec, %2\n\tglobal_load_lds_dwordx4 %0, %1\n\ts_mov_b64 exec, -1" :: "v"(voff), "s"(sbase), "s"(mask) : "memory");
}
template <int N> __device__ __forceinline__ void wait_vm() { asm volatile("s_waitcnt vmcnt(%0) lgkmcnt(0)" :: "i"(N) : "memory"); }
__device__ __forceinline__ void wait_lgkm0() { asm volatile("s_waitcnt lgkmcnt(0)" ::: "memory"); }
__device__ __forceinline__ void bar() { asm volatile("s_barrier" ::: "memory"); }
__device__ __forceinline__ unsigned pk_bf16(float a, float b) { f32x2 p = {a, b}; return __builtin_bit_cast(unsigned, __builtin_convertvector(p, bf16x2)); }
__device__ __forceinline__ u32x2 trd(const LAS char* p) { return __builtin_bit_cast(u32x2, __builtin_amdgcn_ds_read_tr16_b64_v4i16((LAS s16x4*)p)); }
__device__ __forceinline__ bf16x8 b8(u32x4 v) { return __builtin_bit_cast(bf16x8, v); }
__device__ __forceinline__ float bf_lo(unsigned v) { return __uint_as_float(v << 16); }
__device__ __forceinline__ float bf_hi(unsigned v) { return __uint_as_float(v & 0xffff0000u); }
__device__ __forceinline__ float add_f32(float a, float b) { float r; asm("v_add_f32 %0, %1, %2" : "=v"(r) : "v"(a), "v"(b)); return r; }
__device__ __forceinline__ float sub_f32(float a, float b) { float r; asm("v_sub_f32 %0, %1, %2" : "=v"(r) : "v"(a), "v"(b)); return r; }
__device__ __forceinline__ float sel0(float v, u64 m) { float r; asm volatile("v_cndmask_b32 %0, %1, 0, %2" : "=v"(r) : "v"(v), "s"(m)); return r; }
__device__ __forceinline__ float max3f(float a, float b, float c) { return fmaxf(fmaxf(a, b), c); }
__device__ __forceinline__ u64 mix64(u64 z) {
  z += 0x9E3779B97F4A7C15ull; z = (z ^ (z >> 30)) * 0xBF58476D1CE4E5B9ull; z = (z ^ (z >> 27)) * 0x94D049BB133111EBull; return z ^ (z >> 31);
}
template <int CTRL> __device__ __forceinline__ float dpp_add(float v) {
  return v + __builtin_bit_cast(float, __builtin_amdgcn_update_dpp(0, __builtin_bit_cast(int, v), CTRL, 0xf, 0xf, true));
}
__device__ __forceinline__ float half_sum(float v) {
  v = dpp_add<0xB1>(v); v = dpp_add<0x4E>(v); v = dpp_add<0x141>(v); v = dpp_add<0x140>(v);
  auto a = __builtin_amdgcn_permlane16_swap(__float_as_uint(v), __float_as_uint(v), false, false);
  return __uint_as_float(a[0]) + __uint_as_float(a[1]);
}
#define LDSP(T, a) ((LAS T*)(size_t)(a))

#define STAMP(i)
#define TS(i)

__device__ __forceinline__ constexpr int b2grp(int s) {
  return s < 3 ? 16 + s : s < 7 ? 8 + s : s < 18 ? s - 7 : s < 21 ? s - 2 : s < 25 ? s - 10 : s < 28 ? s - 25 : s == 28 ? 15 : 19;
}
struct B2Ctx { const char* wo; const char* w2; int a0, rota, f0, nf, rotf; unsigned voff, vhalf, mhalf, mboth, vq[4], vr[4]; int tb0; };
__device__ __forceinline__ void b2_scalars(B2Ctx& b, const char* wo, const char* w2, unsigned ldsb, int hf, int c, int rt, int ct) {
  b.wo = wo + ct * 128; b.w2 = w2 + ct * 128; b.mboth = ldsb + c * 2048; b.mhalf = b.mboth + hf * 1024;
  b.a0 = 8 * c; b.f0 = c < 3 ? 22 * c : 65; b.nf = c < 2 ? 22 : 21;
  b.rota = ((ct + rt) & 1) * 4; b.rotf = ((ct + rt) & 1) * 10;
}
__device__ __forceinline__ int phys_k16(const B2Ctx& c, int s) {
  if (s < 8) return c.a0 + (s ^ c.rota);
  int t = s - 8 + c.rotf; t = t >= c.nf ? t - c.nf : t;
  return 32 + c.f0 + t;
}
__device__ __forceinline__ const char* b2_src(const B2Ctx& c, int s) {
  return s < 8 ? c.wo + phys_k16(c, s) * 16384 : c.w2 + (phys_k16(c, s) - 32) * 16384;
}
__device__ __forceinline__ void b2_issue(const B2Ctx& c, int s) {
  const char* src = b2_src(c, s);
  set_m0(c.mhalf, b2grp(s) * 8192); nop1();
  glds<0>(c.vhalf, src);
}
__device__ __forceinline__ void b2_issue2(const B2Ctx& c, int s) {
  const char* src = b2_src(c, s);
  set_m0(c.mboth, b2grp(s) * 8192); nop1();
  glds<0>(c.vq[0], src); glds<1024>(c.vr[0], src);
}
struct BRegs { u32x2 lo[10][2], hi[10][2]; };
__device__ __forceinline__ void br_read(BRegs& BR, const LAS char* smem, int tb0, int i) {
  const int s = i >> 2, cb = (i >> 1) & 1;
  if (i & 1) BR.hi[s][cb] = trd(smem + b2grp(s) * 8192 + (tb0 ^ (cb * 64)) + 512); else BR.lo[s][cb] = trd(smem + b2grp(s) * 8192 + (tb0 ^ (cb * 64)));
}

struct Ld { unsigned vo[6]; const char* base0; unsigned mbase; };
struct Epi { f32x4 mp, mc, mp1; unsigned selA, selB; float NEG; float mu[5], rs[5], csv; f32x4 mu4[5], rs4[5], cs[3]; u32x2 b1v[3]; };

template <bool H0, bool QKV, int NS>
struct P1 {
  static constexpr int NRB = H0 ? 5 : 4;
  static constexpr int NL = H0 ? (QKV ? 5 : 4) : 6;
  static constexpr int KSTRIDE = H0 ? 128 : QKV ? 64 * 3072 : 64 * 2752;
  struct Frag { u32x4 A[NRB]; u32x2 Bl[3], Bh[3]; };
  Ld ld; int rot, hdelta; unsigned mbaseh; u64 hmask;
  const char* cb;
  unsigned pb, pa0, pa1;
  u32x4 ones;

  __device__ __forceinline__ void open(int ch) { cb = ld.base0 + ((ch + rot) & 7) * KSTRIDE; set_m0(ld.mbase, (ch % 3) * SLOT); }
  template <int I> __device__ __forceinline__ void ld1(int ch) const {
    if (I >= NL) return;
    if (H0 && I == 4) { set_m0(mbaseh, (ch % 3) * SLOT); glds_mask(ld.vo[4], cb + hdelta, hmask); return; }
    glds<(H0 ? I * 1024 : I * 1024 - 3072)>(ld.vo[I], cb);
  }
  __device__ __forceinline__ void load1(int ch, int i) {
    if (i == 0) { open(ch); nop1(); ld1<0>(ch); } else if (i == 1) ld1<1>(ch); else if (i == 2) ld1<2>(ch);
    else if (i == 3) ld1<3>(ch); else if (i == 4) ld1<4>(ch); else ld1<5>(ch);
  }
  __device__ __forceinline__ void rd(Frag& f, int ks, int n) const {
    constexpr int ord[11] = {0, 1, 6, 2, 3, 4, 5, 7, 8, 9, 10};
    if (n >= 11) return;
    const int i = ord[n];
    if (i < 6) {
      const int s = i >> 1;
      if (s >= NS) return;
      if (i & 1) f.Bh[s] = trd(LDSP(const char, pb) + (32 * ks + 4) * 384 + s * 128); else f.Bl[s] = trd(LDSP(const char, pb) + (32 * ks) * 384 + s * 128);
    } else if (i - 6 < NRB) {
      const int r = H0 ? (i - 5) % 5 : i - 6;
      if (!(H0 && !QKV && r == 0)) f.A[r] = *(const LAS u32x4*)(LDSP(const char, ks ? pa1 : pa0) + r * 2048);
    }
  }
  __device__ __forceinline__ void advance(int delta) {
    asm volatile("v_add_u32 %0, %3, %0\n\tv_add_u32 %1, %3, %1\n\tv_add_u32 %2, %3, %2" : "+v"(pb), "+v"(pa0), "+v"(pa1) : "i"(delta));
  }
  template <bool LAST = false, class F> __device__ __forceinline__ void mma(f32x4 (&acc)[5][3], Frag& f, F&& fill) const {
#pragma unroll
    for (int rr = 0; rr < NRB; rr++) {
      const int r = !H0 ? rr : LAST ? (rr + 4) % 5 : (rr + 1) % 5;
#pragma unroll
      for (int s = 0; s < 3; s++) {
        if (s < NS) {
          const u32x4 B = {f.Bl[s].x, f.Bl[s].y, f.Bh[s].x, f.Bh[s].y};
          const u32x4 A = (H0 && !QKV && r == 0) ? ones : f.A[r];
          if (QKV && s == 2) acc[r][s] = __builtin_amdgcn_mfma_f32_16x16x32_bf16(b8(A), b8(B), acc[r][s], 0, 0, 0);
          else acc[r][s] = __builtin_amdgcn_mfma_f32_16x16x32_bf16(b8(B), b8(A), acc[r][s], 0, 0, 0);
          SB();
        }
        fill(3 * rr + s);
        SB();
      }
    }
  }
  template <class F> __device__ __forceinline__ void stepA(f32x4 (&acc)[5][3], Frag& cur, Frag& nx, int delta, F&& site) {
    mma(acc, cur, [&](int n) {
      rd(nx, 1, n);
      constexpr int S0 = H0 ? 4 : 2;
      if (n >= S0 && (n - S0) % 4 == 0 && (n - S0) / 4 < 3) site((n - S0) / 4);
      if (n == 3 * NRB - 2 && delta != 0) advance(delta);
    });
  }
  template <class S, class F> __device__ __forceinline__ void stepB(f32x4 (&acc)[5][3], Frag& cur, Frag& nx, S&& sync, F&& site) const {
    mma(acc, cur, [&](int n) {
      if (n == BG - 1) sync();
      if (n >= BG - 1) { const int i = n - (BG - 1); if (i < 2) { rd(nx, 0, 2 * i); rd(nx, 0, 2 * i + 1); } else rd(nx, 0, i + 2); }
      constexpr int S0 = H0 ? 6 : 5, SD = H0 ? 4 : 3;
      if (n >= S0 && (n - S0) % SD == 0 && (n - S0) / SD < 3) site((n - S0) / SD);
    });
  }
};

template <bool H0, bool QKV, int NSB>
__device__ __forceinline__ void phase1(const Params& P, LAS char* smem, unsigned ldsb, const Ld& ld, int p, int j, int c, int rot, int rt, int ct,
                                       B2Ctx& b2c, BRegs& BR, u64& tag, u64 (&stamps)[NSTAMP], unsigned& tsv) {
  typedef P1<H0, QKV, NSB> T;
  constexpr int NL = T::NL, HF = H0 ? 0 : 1;
  const int lane = threadIdx.x & 63;
  f32x4 acc[5][3]; Epi ep;
  T t;
  t.ld = ld; t.rot = rot;
  t.mbaseh = ldsb + 4 * c * 128; t.hdelta = p > 0 ? -16384 : 0; t.hmask = c == 0 ? 0xffff0000ull : 0xffffffffull;
  u64 e_old;
  {
    const int g = lane >> 4, m = lane & 15, q4 = m >> 2, pp = lane & 3;
    t.pa0 = ldsb + (H0 ? 0 : 5 * 2048) + m * 128 + ((g ^ (m >> 1)) * 16); t.pa1 = t.pa0 ^ 64;
    t.pb = ldsb + XB + (8 * g + q4) * 384 + (((2 * c + (pp >> 1)) ^ (2 * ((q4 >> 1) | ((g & 1) << 1)))) * 16) + (pp & 1) * 8;
    const unsigned o = m == 0 ? ONES : 0u; t.ones = (u32x4){o, o, o, o};
    if (H0 && !QKV) asm volatile("" : "+v"(t.ones));
    if (H0 && QKV) { if (c == 0 && lane < 48) *(LAS u32x4*)(smem + g * SLOT + m * 16) = (u32x4){ONES, ONES, ONES, ONES}; }
  }
#pragma unroll
  for (int r = 0; r < 5; r++)
#pragma unroll
    for (int s = 0; s < 3; s++) acc[r][s] = (f32x4){0.f, 0.f, 0.f, 0.f};
  typename T::Frag F0, F1;
  constexpr int NX = H0 ? 2 : 0, NSS = H0 ? 1 : 0;
  const int srow = p * 128 + ((QKV ? 0 : 8) + j) * 8 + 2 * c;
  u32x4 xr0, xr1; float xv[16], mean, sq; unsigned ssa;
  auto row_stats = [&](int k) {
    if (k == 0) {
#pragma unroll
      for (int e = 0; e < 4; e++) { xv[2 * e] = bf_lo(xr0[e]); xv[2 * e + 1] = bf_hi(xr0[e]); xv[8 + 2 * e] = bf_lo(xr1[e]); xv[9 + 2 * e] = bf_hi(xr1[e]); }
      float s0 = xv[0], s1 = xv[8];
#pragma unroll
      for (int e = 1; e < 8; e++) { s0 = add_f32(s0, xv[e]); s1 = add_f32(s1, xv[8 + e]); }
      mean = s0 + s1;
      asm volatile("" : "+v"(mean));
    }
    if (k == 1) { mean = half_sum(mean) * (1.f / 512.f); asm volatile("" : "+v"(mean)); }
    if (k == 2) {
      float q0 = 0.f, q1 = 0.f;
#pragma unroll
      for (int e = 0; e < 8; e++) { float d = sub_f32(xv[e], mean), d2 = sub_f32(xv[8 + e], mean); q0 = fmaf(d, d, q0); q1 = fmaf(d2, d2, q1); asm volatile("" : "+v"(q0), "+v"(q1)); }
      sq = q0 + q1;
      asm volatile("" : "+v"(sq));
    }
    if (k == 3) { sq = __builtin_amdgcn_rsqf(half_sum(sq) * (1.f / 512.f) + 1e-5f); asm volatile("" : "+v"(sq)); }
    if (k == 4) {
      const u32x2 gr = {__float_as_uint((lane & 1) ? sq : mean), (unsigned)tag};
      asm volatile("s_mov_b64 exec, %3\n\tglobal_store_dwordx2 %0, %1, %2 sc1\n\ts_mov_b64 exec, -1" :: "v"(ssa), "v"(gr), "s"(P.ws + WS_STAT), "s"(0x0000000300000003ull) : "memory");
    }
  };
  if (H0) {
    if (!XF) asm volatile("global_load_dwordx4 %0, %2, %3\n\tglobal_load_dwordx4 %1, %2, %3 offset:512" : "=&v"(xr0), "=&v"(xr1) : "v"((lane >> 5) * 1024 + (lane & 31) * 16), "s"(P.x + srow * 1024) : "memory");
    ssa = (srow + (lane >> 5)) * 16 + (lane & 1) * 8; asm volatile("" : "+v"(ssa));
  }
  asm volatile("s_load_dwordx2 %0, %1, 0x0" : "=s"(e_old) : "s"(P.ws) : "memory");
  unsigned pfd = 0, pfo; const char* pfw;
  auto warm = [&](int k) {
    asm volatile("s_mov_b64 exec, %3\n\tglobal_load_dword %0, %1, %2\n\ts_mov_b64 exec, -1" : "+v"(pfd) : "v"(pfo), "s"(pfw), "s"(0x3ull << (2 * k)) : "memory");
  };
  unsigned wpo; const char* wpw;
  constexpr int NW = (H0 && WPF) ? 1 : 0;
  auto w_warm = [&]() { asm volatile("global_load_dword %0, %1, %2" : "+v"(pfd) : "v"(wpo), "s"(wpw) : "memory"); };
  if (NW) {
    const int w0 = blockIdx.x * 91 + c * 23, wl = c == 3 ? 21 : 22, wn = 12288 - w0;
    wpw = w0 < 12288 ? P.wqkv + w0 * 128 : P.w1 + (w0 - 12288) * 128;
    const int wt = (w0 < 12288 && wn - 1 < wl) ? wn - 1 : wl;
    wpo = (lane < wt ? lane : wt) * 128; asm volatile("" : "+v"(wpo), "+s"(wpw));
    if (WPF == 3) w_warm();
  }
  unsigned sto, stw, str1, str4, csw, csw1, kvw, vq[4], vr[4];
  const char *bA, *bB, *bC;
  auto later = [&](int k) {
    const int g = lane >> 4, m = lane & 15, w = 4 * HF + c;
    if (k == 0) {
      u64 z = mix64(e_old); unsigned lo = __builtin_amdgcn_readfirstlane((unsigned)z), hi = __builtin_amdgcn_readfirstlane((unsigned)(z >> 32));
      asm volatile("" : "+s"(lo), "+s"(hi)); tag = ((u64)hi << 32) | lo;
    }
    if (k == 1) {
      const int l0 = blockIdx.x * 59 + w * 8, lim = blockIdx.x * 59 + 58 - l0;
      const int n0 = 4096 - l0;
      pfw = l0 < 4096 ? P.wo + l0 * 128 : P.w2 + (l0 - 4096) * 128;
      const int top = (l0 < 4096 && n0 - 1 < lim) ? n0 - 1 : lim;
      const int l = lane & 7;
      pfo = (l < top ? l : top) * 128; asm volatile("" : "+v"(pfo), "+s"(pfw));
      const int sti = 18 * w + (lane < 18 ? lane : 17), row = p * 128 - 16 + sti;
      sto = (row < 0 ? 0 : row) * 16; stw = ldsb + ST_MEAN + sti * 4; str1 = ldsb + ST_MEAN + (64 * HF + m) * 4;
      str4 = QKV ? ldsb + ST_MEAN + (64 * HF + 4 * g) * 4 : ldsb + KVX + (c * 16 + 4 * g) * 2;
      csw = ldsb + ST_CS + (c * 16 + 4 * g) * 4; csw1 = ldsb + ST_CS + (128 + c * 16 + m) * 4; kvw = ldsb + KVX + c * 2048 + lane * 16;
      asm volatile("" : "+v"(sto), "+v"(stw), "+v"(str1), "+v"(str4), "+v"(csw));
      if (QKV) asm volatile("" : "+v"(csw1), "+v"(kvw));
      b2c.voff = (lane >> 3) * 1024 + (((lane & 7) ^ ((g & 1) << 2)) * 16);
#pragma unroll
      for (int i = 0; i < 4; i++) vq[i] = b2c.voff + i * 16384;
      b2c.vhalf = b2c.voff + HF * 8192;
      b2c.tb0 = c * 2048 + (8 * (g >> 1) + (m >> 2)) * 128 + (((2 * (((g & 1) + c) & 3) + ((lane & 3) >> 1)) ^ ((m >> 3) << 2)) * 16) + (lane & 1) * 8;
      asm volatile("" : "+v"(b2c.vhalf), "+v"(b2c.tb0));
      if (H0) {
#pragma unroll
        for (int i = 0; i < 4; i++) { vr[i] = vq[i] + 7168; asm volatile("" : "+v"(vq[i]), "+v"(vr[i])); b2c.vq[i] = vq[i]; b2c.vr[i] = vr[i]; }
      }
      if (QKV) {
        constexpr u64 MP[4] = {0x555005500050000ull, 0xaaa00aa000a0000ull, 0x1555015500150001ull, 0x2aaa02aa002a0002ull}, MC[4] = {0x5000550055505555ull, 0xa000aa00aaa0aaaaull, 0x4000540055405554ull, 0x8000a800aa80aaa8ull};
        ep.NEG = -3.0e38f; asm volatile("" : "+v"(ep.NEG));
        const u64 live = (H0 && p == 0) ? 0ull : ~0ull;
#pragma unroll
        for (int e = 0; e < 4; e++) {
          ep.mp[e] = sel0(ep.NEG, MP[e]); ep.mc[e] = sel0(ep.NEG, MC[e]);
          ep.mp1[e] = H0 ? sel0(ep.NEG, MP[e] & live) : ep.mp[e];
        }
        ep.selA = (m & 1) ? 0x01000c0cu : 0x0c0c0100u; ep.selB = ep.selA + ((m & 1) ? 0x02020000u : 0x00000202u);
        asm volatile("" : "+v"(ep.selA), "+v"(ep.selB));
      }
    }
    if (k == 2 && H0) {
      b2_scalars(b2c, P.wo, P.w2, ldsb, HF, c, rt, ct);
      bA = b2_src(b2c, 0); asm volatile("" : "+s"(bA), "+s"(b2c.mboth));
    }
    if (k == 3 && H0) { bB = b2_src(b2c, 4); asm volatile("" : "+s"(bB)); }
    if (k == 4 && H0) { bC = b2_src(b2c, 8); asm volatile("" : "+s"(bC)); }
    if (k == 5 && H0) { if (lane == 0) *LDSP(unsigned, ldsb + PAIRF + c * 4) = 0u; }
  };
  auto b2_early = [&](int i) {
    if (H0) { const char* src = i < 4 ? bA : i < 8 ? bB : bC; set_m0(b2c.mboth, b2grp(i) * 8192); nop1(); glds<0>(vq[i & 3], src); glds<1024>(vr[i & 3], src); }
  };
  later(1);
  SB();
  TS(63);
  if (NW && WPF == 4) w_warm();
  constexpr int NWE = (WPF == 3 || WPF == 4) ? NW : 0;
  if (!(H0 && SBC)) {
#pragma unroll
    for (int i = 0; i < 6; i++) t.load1(1, i);
  }
  asm volatile("s_waitcnt lgkmcnt(0)" : "+s"(e_old) :: "memory");
  later(0);
  if (H0) {
    wait_vm<(SBC ? NL : 2 * NL) + NWE>();
    TS(2);
    { const LAS char* q = smem + 16 * 8192 + c * 2048 + (lane >> 5) * 1024 + (lane & 31) * 16; xr0 = *(const LAS u32x4*)q; xr1 = *(const LAS u32x4*)(q + 512); }
#pragma unroll
    for (int k = 0; k < 5; k++) row_stats(k);
    if (SBC) {
#pragma unroll
      for (int i = 0; i < 6; i++) t.load1(1, i);
    }
  }
  SB();
  TS(3);
  wait_vm<NL + NSS + NWE>();
  if (NW && WPF == 1) w_warm();
  TS(4);
  bar();
  TS(5);
  STAMP(1);
#pragma unroll
  for (int n = 0; n < 11; n++) t.rd(F0, 0, n);
  u32x4 st;
  t.stepA(acc, F0, F1, SLOT, [&](int k) { t.load1(2, 2 * k); t.load1(2, 2 * k + 1); if (NW && WPF == 2 && k == 2) w_warm(); });
  t.stepB(acc, F1, F0, [&]() { TS(6); wait_vm<NL + (SBC ? 0 : NSS) + NW - NWE>(); TS(7); bar(); TS(8); }, [&](int k) { t.load1(3, k); });
#pragma unroll
  for (int ch = 1; ch < 5; ch++) {
    t.stepA(acc, F0, F1, ch % 3 == 2 ? -2 * SLOT : SLOT, [&](int k) {
      if (H0 && !XF && ch == 2 && k == 0) row_stats(3);
      t.load1(ch + 2, 3 + k);
      if (k == 2) { warm(ch - 1); if (H0 && !XF && ch == 2) row_stats(4); }
    });
    t.stepB(acc, F1, F0, [&]() {
      TS(6 + 3 * ch);
      if (ch == 1) { if (H0 && !XF) asm volatile("s_waitcnt vmcnt(%2) lgkmcnt(0)" : "+v"(xr0), "+v"(xr1) : "i"(NL + 1 + (WPF == 2 ? NW : 0)) : "memory"); else wait_vm<NL + 1 + (WPF == 2 ? NW : 0)>(); }
      else if (ch < 4) wait_vm<NL + 2 + (XF ? 0 : NSS)>();
      else wait_vm<NL + 2>();
      TS(7 + 3 * ch); bar(); TS(8 + 3 * ch);
      if (STE && ch == 4) asm volatile("global_load_dwordx4 %0, %1, %2 sc1" : "=v"(st) : "v"(sto), "s"(P.ws + WS_STAT) : "memory");
    }, [&](int k) {
      t.load1(ch + 3, k);
      if (H0 && !XF && ch == 1) row_stats(k);
      if (k == 1) later(1 + ch);
    });
  }
  auto stats_to_lds = [&]() {
    const unsigned tg = (unsigned)tag;
    if (!__all(st.y == tg && st.w == tg)) {
      const GAS u64* sp = (const GAS u64*)(P.ws + WS_STAT + sto);
#pragma unroll 1
      for (unsigned spin = 0; spin < SPIN_CAP; spin++) {
        u64 a = __hip_atomic_load(sp, __ATOMIC_RELAXED, __HIP_MEMORY_SCOPE_AGENT), b = __hip_atomic_load(sp + 1, __ATOMIC_RELAXED, __HIP_MEMORY_SCOPE_AGENT);
        st = (u32x4){(unsigned)a, (unsigned)(a >> 32), (unsigned)b, (unsigned)(b >> 32)};
        if (__all(st.y == tg && st.w == tg)) break;
        __builtin_amdgcn_s_sleep(1);
      }
    }
    *LDSP(unsigned, stw) = st.x; *LDSP(unsigned, stw + 576) = st.z;
  };
  auto row_ops = [&](int r) {
    ep.mu[r] = *LDSP(const float, str1 + 64 * r); ep.rs[r] = *LDSP(const float, str1 + 64 * r + 576);
    if (QKV) { ep.mu4[r] = *LDSP(const f32x4, str4 + 64 * r); ep.rs4[r] = *LDSP(const f32x4, str4 + 64 * r + 576); }
    else if (r < 4) ep.b1v[r - 1] = *LDSP(const u32x2, str4 + 128 * (r - 1));
  };
  t.stepA(acc, F0, F1, -2 * SLOT, [&](int k) { t.load1(7, 3 + k); });
  t.stepB(acc, F1, F0, [&]() {
    TS(21); asm volatile("s_waitcnt vmcnt(%1) lgkmcnt(0)" : "+v"(pfd) : "i"(NL + STE) : "memory"); TS(22); bar(); TS(23);
    if (!STE) asm volatile("global_load_dwordx4 %0, %1, %2 sc1" : "=v"(st) : "v"(sto), "s"(P.ws + WS_STAT) : "memory");
  }, [&](int k) { b2_early(k); });
  t.stepA(acc, F0, F1, SLOT, [&](int k) {
    b2_early(3 + k);
    if (STE && k == 2) { asm volatile("s_waitcnt vmcnt(%1)" : "+v"(st) : "i"(NL + (H0 ? 12 : 0)) : "memory"); stats_to_lds(); }
  });
  t.stepB(acc, F1, F0, [&]() { TS(24); wait_vm<(H0 ? 12 : 0) + 1 - STE>(); TS(25); bar(); TS(26); }, [&](int k) { b2_early(6 + k); });
  t.stepA(acc, F0, F1, 0, [&](int k) { b2_early(9 + k); });
  TS(27);
  t.template mma<true>(acc, F1, [&](int n) {
    if (STE) { const int r = n + (QKV ? 0 : 1); if (r < 5) row_ops(r); }
    else if (n == 1) { asm volatile("s_waitcnt vmcnt(%1)" : "+v"(st) : "i"(H0 ? 24 : 0) : "memory"); stats_to_lds(); }
    if (H0 && QKV && n == 6) { *LDSP(f32x4, kvw) = acc[4][1]; *LDSP(f32x4, kvw + 1024) = acc[4][2]; }
    if (H0 && n == 9) {
      if (QKV) { if (lane < 16) *LDSP(float, csw1) = acc[0][2][0]; }
      if ((lane & 15) == 0) {
        *LDSP(f32x4, csw) = acc[0][0];
        *LDSP(f32x4, csw + 256) = acc[0][1];
        if (!QKV) *LDSP(f32x4, csw + 512) = acc[0][2];
      }
    }
  });
  STAMP(2);
  if (!H0) {
#pragma unroll
    for (int r = 4; r >= 1; r--)
#pragma unroll
      for (int s = 0; s < 3; s++) acc[r][s] = acc[r - 1][s];
  }
  TS(29);
  wait_vm<H0 ? 24 - 2 * NE : 0>();
  if (PRIO == 1) { if (H0) __builtin_amdgcn_s_setprio(2); else __builtin_amdgcn_s_setprio(1); }
  if (PRIO == 2) { if (!H0) __builtin_amdgcn_s_setprio(1); }
  bar();
  TS(31);
  ep.cs[0] = *LDSP(const f32x4, csw); ep.cs[1] = *LDSP(const f32x4, csw + 256);
  if (QKV) ep.csv = *LDSP(const float, csw1); else ep.cs[2] = *LDSP(const f32x4, csw + 512);
  if (!H0 && QKV) { acc[0][1] = *LDSP(const f32x4, kvw); acc[0][2] = *LDSP(const f32x4, kvw + 1024); }
  if (!STE) {
#pragma unroll
    for (int r = QKV ? 0 : 1; r < 5; r++) row_ops(r);
  }
  SB();
  STAMP(3);
  constexpr int hf = HF; constexpr bool qkv = QKV, blk2 = NSB == 3;
  const __amdgpu_buffer_rsrc_t act = __builtin_amdgcn_make_buffer_rsrc(P.ws + WS_ACT, 0, ACT_BYTES, 0x00020000);
  const int rt32 = p * 4 + hf * 2, tb0 = b2c.tb0;
  const char* bD = bC + 65536;
  auto side = [&](int u) {
    SB();
    if (hf == 0) {
      if (!DSIDE) return;
      const char* src = u < 4 ? bD : bD + 65536;
      set_m0(b2c.mboth, b2grp(12 + u) * 8192); nop1(); glds<0>(b2c.vq[u & 3], src); glds<1024>(b2c.vr[u & 3], src);
    } else {
      if (u < 4) {
#pragma unroll
        for (int i = 0; i < NE; i++) br_read(BR, smem, tb0, NE * u + i);
      }
      if (u == 5) { wait_lgkm0(); if (lane == 0) *(LAS unsigned*)(smem + PAIRF + c * 4) = 1u; }
    }
    SB();
  };
  if (qkv) {
    u32x2 qf[5], kf[5], vf[5];
#pragma unroll
    for (int r = 0; r < 5; r++) {
      f32x4 k4 = (acc[r][1] - ep.mu[r] * ep.cs[1]) * ep.rs[r], v4 = (acc[r][2] - ep.mu4[r] * ep.csv) * ep.rs4[r];
      kf[r] = (u32x2){pk_bf16(k4[0], k4[1]), pk_bf16(k4[2], k4[3])};
      vf[r] = (u32x2){pk_bf16(v4[0], v4[1]), pk_bf16(v4[2], v4[3])};
      if (r > 0) { f32x4 q4v = (acc[r][0] - ep.mu[r] * ep.cs[0]) * ep.rs[r]; qf[r] = (u32x2){pk_bf16(q4v[0], q4v[1]), pk_bf16(q4v[2], q4v[3])}; }
      side(r);
    }
    TS(32);
    const float SC = 0.25f * 1.4426950408889634f, NEG = ep.NEG;
    float sv[4][4], mx[4];
#pragma unroll
    for (int r = 1; r < 5; r++) {
      f32x4 sp = __builtin_amdgcn_mfma_f32_16x16x16bf16_1k(__builtin_bit_cast(s16x4, kf[r - 1]), __builtin_bit_cast(s16x4, qf[r]), r == 1 ? ep.mp1 : ep.mp, 0, 0, 0);
      f32x4 sc = __builtin_amdgcn_mfma_f32_16x16x16bf16_1k(__builtin_bit_cast(s16x4, kf[r]), __builtin_bit_cast(s16x4, qf[r]), ep.mc, 0, 0, 0);
      sv[r - 1][0] = max3f(sp[0], sp[1], NEG); sv[r - 1][1] = max3f(sp[2], sp[3], NEG); sv[r - 1][2] = max3f(sc[0], sc[1], NEG); sv[r - 1][3] = max3f(sc[2], sc[3], NEG);
      mx[r - 1] = max3f(max3f(sv[r - 1][0], sv[r - 1][1], sv[r - 1][2]), sv[r - 1][3], NEG);
    }
    side(5);
    TS(36);
    {
      auto a = __builtin_amdgcn_permlane16_swap(__float_as_uint(mx[0]), __float_as_uint(mx[1]), false, false);
      auto b = __builtin_amdgcn_permlane16_swap(__float_as_uint(mx[2]), __float_as_uint(mx[3]), false, false);
      const float u = max3f(__uint_as_float(a[0]), __uint_as_float(a[1]), NEG), v = max3f(__uint_as_float(b[0]), __uint_as_float(b[1]), NEG);
      auto d = __builtin_amdgcn_permlane32_swap(__float_as_uint(u), __float_as_uint(v), false, false);
      const unsigned t = __float_as_uint(max3f(__uint_as_float(d[0]), __uint_as_float(d[1]), NEG) * SC);
      auto e = __builtin_amdgcn_permlane32_swap(t, t, false, false);
      auto f0 = __builtin_amdgcn_permlane16_swap(e[0], e[0], false, false);
      auto f1 = __builtin_amdgcn_permlane16_swap(e[1], e[1], false, false);
      mx[0] = __uint_as_float(f0[0]); mx[1] = __uint_as_float(f0[1]); mx[2] = __uint_as_float(f1[0]); mx[3] = __uint_as_float(f1[1]);
    }
    const u32x4 one4 = {ONES, ONES, ONES, ONES};
    const f32x4 z = {0.f, 0.f, 0.f, 0.f};
    f32x4 o4[4], l4[4];
#pragma unroll
    for (int r = 0; r < 4; r++) {
      float e[4];
#pragma unroll
      for (int i = 0; i < 4; i++) e[i] = __builtin_amdgcn_exp2f(sv[r][i] * SC - mx[r]);
      const unsigned pa = pk_bf16(e[0], e[1]), pb = pk_bf16(e[2], e[3]);
      const u32x4 pf = {__builtin_amdgcn_perm(0u, pa, ep.selA), __builtin_amdgcn_perm(0u, pa, ep.selB), __builtin_amdgcn_perm(0u, pb, ep.selA), __builtin_amdgcn_perm(0u, pb, ep.selB)};
      const u32x4 vv = {vf[r].x, vf[r].y, vf[r + 1].x, vf[r + 1].y};
      o4[r] = __builtin_amdgcn_mfma_f32_16x16x32_bf16(b8(vv), b8(pf), z, 0, 0, 0);
      l4[r] = __builtin_amdgcn_mfma_f32_16x16x32_bf16(b8(one4), b8(pf), z, 0, 0, 0);
    }
    u32x2 of[4];
#pragma unroll
    for (int r = 0; r < 4; r++) {
      const f32x4 o = o4[r] * __builtin_amdgcn_rcpf(l4[r][0]);
      of[r] = (u32x2){pk_bf16(o[0], o[1]), pk_bf16(o[2], o[3])};
    }
#pragma unroll
    for (int pr = 0; pr < 2; pr++) {
      auto s0 = __builtin_amdgcn_permlane16_swap(of[2 * pr].x, of[1 + 2 * pr].x, false, false);
      auto s1 = __builtin_amdgcn_permlane16_swap(of[2 * pr].y, of[1 + 2 * pr].y, false, false);
      __builtin_amdgcn_raw_buffer_store_b128((u32x4){s0[0], s1[0], s0[1], s1[1]}, act, lane * 16 + ((rt32 + pr) * NK16 + 4 * j + c) * 1024, 0, 16);
    }
    SB();
  } else {
    const unsigned sto0 = lane * 16 + (rt32 * NK16 + 32 + 11 * j + c) * 1024;
    auto gelu_block = [&](int s) {
      const f32x4 bb = {bf_lo(ep.b1v[s].x), bf_hi(ep.b1v[s].x), bf_lo(ep.b1v[s].y), bf_hi(ep.b1v[s].y)};
      f32x4 v[4], z[4];
#pragma unroll
      for (int r = 0; r < 4; r++) v[r] = acc[r + 1][s] - ep.mu[r + 1] * ep.cs[s];
      SB();
#pragma unroll
      for (int r = 0; r < 4; r++) v[r] = v[r] * ep.rs[r + 1] + bb;
      SB();
#pragma unroll
      for (int r = 0; r < 4; r++) z[r] = v[r] * v[r];
      if (s < 2) side(3 * s); else SB();
#pragma unroll
      for (int r = 0; r < 4; r++) z[r] = z[r] * (-0.10294324f) + (-2.3022082f);
      SB();
#pragma unroll
      for (int r = 0; r < 4; r++) z[r] = z[r] * v[r];
      SB();
#pragma unroll
      for (int r = 0; r < 4; r++) z[r] = (f32x4){__builtin_amdgcn_exp2f(z[r][0]), __builtin_amdgcn_exp2f(z[r][1]), __builtin_amdgcn_exp2f(z[r][2]), __builtin_amdgcn_exp2f(z[r][3])};
      if (s < 2) side(3 * s + 1); else SB();
#pragma unroll
      for (int r = 0; r < 4; r++) z[r] = z[r] + 1.f;
      SB();
#pragma unroll
      for (int r = 0; r < 4; r++) z[r] = (f32x4){__builtin_amdgcn_rcpf(z[r][0]), __builtin_amdgcn_rcpf(z[r][1]), __builtin_amdgcn_rcpf(z[r][2]), __builtin_amdgcn_rcpf(z[r][3])};
      if (s < 2) side(3 * s + 2); else SB();
#pragma unroll
      for (int r = 0; r < 4; r++) v[r] = v[r] * z[r];
      SB();
      u32x2 hfr[4];
#pragma unroll
      for (int r = 0; r < 4; r++) hfr[r] = (u32x2){pk_bf16(v[r][0], v[r][1]), pk_bf16(v[r][2], v[r][3])};
      SB();
#pragma unroll
      for (int pr = 0; pr < 2; pr++) {
        auto s0 = __builtin_amdgcn_permlane16_swap(hfr[2 * pr].x, hfr[2 * pr + 1].x, false, false);
        auto s1 = __builtin_amdgcn_permlane16_swap(hfr[2 * pr].y, hfr[2 * pr + 1].y, false, false);
        __builtin_amdgcn_raw_buffer_store_b128((u32x4){s0[0], s1[0], s0[1], s1[1]}, act, sto0 + (pr * NK16 + 4 * s) * 1024, 0, 16);
      }
      SB();
      TS(32 + s);
    };
    gelu_block(0);
    gelu_block(1);
    if (blk2) gelu_block(2);
  }
  STAMP(4);
  TS(40);
  if (hf == 0) {
    __builtin_amdgcn_s_setprio(0);
    if (!DSIDE) {
#pragma unroll
      for (int u = 0; u < 6; u++) { const char* src = u < 4 ? bD : bD + 65536; set_m0(b2c.mboth, b2grp(12 + u) * 8192); nop1(); glds<0>(b2c.vq[u & 3], src); glds<1024>(b2c.vr[u & 3], src); }
    }
#pragma unroll
    for (int i = 0; i < 4 * NE; i++) br_read(BR, smem, tb0, i);
#pragma unroll 1
    for (unsigned spin = 0; spin < SPIN_CAP; spin++)
      if (__builtin_amdgcn_readfirstlane(__hip_atomic_load((LAS unsigned*)(smem + PAIRF + c * 4), __ATOMIC_RELAXED, __HIP_MEMORY_SCOPE_WORKGROUP)) == 1u) break;
    wait_lgkm0();
#pragma unroll
    for (int s = 18; s < 18 + NE; s++) b2_issue2(b2c, s);
    TS(44);
    wait_vm<2 * NE + (DSIDE ? 0 : 12)>();
  } else {
    wait_vm<0>();
    __builtin_amdgcn_s_setprio(0);
  }
}

typedef __attribute__((ext_vector_type(16))) unsigned u32x16;
__device__ __forceinline__ char* mkptr(unsigned lo, unsigned hi) { return (char*)(((u64)hi << 32) | lo); }
__global__ __launch_bounds__(512, 2) void fused_block(Params) {
  u64 stamps[NSTAMP];
  unsigned tsv = 0;
  TS(0);
  STAMP(0);
  u32x16 ka; u32x2 kb;
  asm volatile("s_load_dwordx16 %0, %2, 0x0\n\ts_load_dwordx2 %1, %2, 0x40" : "=&s"(ka), "=&s"(kb) : "s"(__builtin_amdgcn_kernarg_segment_ptr()));
  Params P;
  auto args = [&]() {
    asm volatile("s_waitcnt lgkmcnt(0)" : "+s"(ka), "+s"(kb));
    P.x = mkptr(ka[0], ka[1]); P.wqkv = mkptr(ka[2], ka[3]); P.wo = mkptr(ka[4], ka[5]); P.w1 = mkptr(ka[6], ka[7]); P.b1 = mkptr(ka[8], ka[9]);
    P.w2 = mkptr(ka[10], ka[11]); P.b2 = mkptr(ka[12], ka[13]); P.out = mkptr(ka[14], ka[15]); P.ws = mkptr(kb[0], kb[1]);
  };
  __shared__ __attribute__((aligned(16))) char smem_[163840];
  LAS char* smem = (LAS char*)smem_;
  const unsigned ldsb = (unsigned)(size_t)smem;
  const int tid = threadIdx.x, lane = tid & 63, wave = __builtin_amdgcn_readfirstlane(tid >> 6);
  const int b = blockIdx.x, xcd = b & 7, r5 = b >> 3;
  unsigned cpd = 0;
  if (CPF == 1) {
    u64 pc; asm volatile("s_getpc_b64 %0" : "=s"(pc)); pc &= ~255ull;
    asm volatile("global_load_dword %0, %1, %2" : "+v"(cpd) : "v"(((tid < CODE_LINES / 32 ? tid : 0) * 32 + r5) * 128), "s"(pc) : "memory");
  }
  const int hf = wave >> 2, c = wave & 3, jj = r5 >> 3;
  const bool qkv = jj < 2;
  const int p = (xcd & 1) * 8 + (r5 & 7), j = (xcd >> 1) * 2 + (jj & 1);
  const int rot1 = ROT1_MODE == 0 ? 0 : ROT1_MODE == 1 ? (p + jj) : ROT1_MODE == 2 ? ((p + jj) & 1) * 4 : ((p + jj) & 3) * 2;
  const int rt = xcd * 4 + jj, ct = r5 & 7;
  Ld ld;
  const int pitch = qkv ? 3072 : 2752, k0 = rot1 & 7;
  int so0, ko0;
  if (hf == 0) {
    so0 = p * 131072 - 4096; ko0 = k0 * 128; ld.mbase = ldsb + (16 + 32 * c) * 128;
    asm volatile("" : "+s"(so0), "+s"(ko0), "+s"(ld.mbase));
    const unsigned v0 = 4096 + (32 * c + (lane >> 3)) * 1024 + (((lane & 7) ^ (lane >> 4)) * 16);
    ld.vo[0] = v0; ld.vo[1] = (v0 ^ 64) + 7168; ld.vo[2] = v0 + 14336; ld.vo[3] = (v0 ^ 64) + 21504;
    { const int row = 4 * c + ((lane >> 3) & 3); ld.vo[4] = 4096 + row * 1024 + (((lane & 7) ^ (row >> 1)) * 16); }
    ld.vo[5] = (lane < (j == 7 ? 18 : 22) ? lane : 0) * 16;
  } else {
    const int secst = qkv ? 1024 : 128, lim = qkv ? 23 : (j == 7 ? 17 : 21);
    so0 = j * (qkv ? 128 : 352) + c * 16 * pitch - 4096; ko0 = k0 * 64 * pitch; ld.mbase = ldsb + XB + c * 6144 + 3072;
    asm volatile("" : "+s"(so0), "+s"(ko0), "+s"(ld.mbase));
#pragma unroll
    for (int r = 0; r < 3; r++) {
      const int v = lane + 64 * r, row = (v * 171) >> 12, cc = (v - 24 * row) ^ (row & 2);
      const int c0 = cc < lim ? cc : lim, c1 = (cc ^ 4) < lim ? (cc ^ 4) : lim;
      ld.vo[r] = 4096 + 3072 - 1024 * r + row * pitch + (c0 >> 3) * secst + (c0 & 7) * 16;
      ld.vo[r + 3] = 4096 - 1024 * r + (row + 8) * pitch + (c1 >> 3) * secst + (c1 & 7) * 16;
    }
  }
  unsigned xvo = lane * 16;
  asm volatile("" : "+v"(ld.vo[0]), "+v"(ld.vo[1]), "+v"(ld.vo[2]), "+v"(ld.vo[3]), "+v"(ld.vo[4]), "+v"(ld.vo[5]), "+v"(xvo));
  SB();
  args();
  ld.base0 = (hf == 0 ? P.x : qkv ? P.wqkv : P.w1) + so0;
  TS(62);
  if (hf == 0) {
    const u64 hmask = c == 0 ? 0xffff0000ull : 0xffffffffull;
    if (XF) {
      const char* xrow = P.x + (p * 128 + ((qkv ? 0 : 8) + j) * 8 + 2 * c) * 1024;
      set_m0(ldsb + c * 2048, 16 * 8192); nop1(); glds<0>(xvo, xrow); glds<1024>(xvo, xrow);
    }
    const char* cb = ld.base0 + ko0;
    set_m0(ld.mbase, 0); nop1();
    glds<0>(ld.vo[0], cb); glds<1024>(ld.vo[1], cb); glds<2048>(ld.vo[2], cb); glds<3072>(ld.vo[3], cb);
    if (qkv) { set_m0(ldsb + 4 * c * 128, 0); glds_mask(ld.vo[4], cb + (p > 0 ? -16384 : 0), hmask); }
    if (!qkv && c == 0) { set_m0(ldsb, KVX); nop1(); glds<0>(ld.vo[5], P.b1 + j * 352); }
  } else {
    const char* cb = ld.base0 + ko0;
    set_m0(ld.mbase, 0); nop1();
    glds<-3072>(ld.vo[0], cb); glds<-2048>(ld.vo[1], cb); glds<-1024>(ld.vo[2], cb); glds<0>(ld.vo[3], cb); glds<1024>(ld.vo[4], cb); glds<2048>(ld.vo[5], cb);
  }
  SB();
  TS(1);
  B2Ctx b2c; BRegs BR; u64 tag;
#define P1ARGS P, smem, ldsb, ld, p, j, c, rot1, rt, ct, b2c, BR, tag, stamps, tsv
  const bool blk2 = 8 + c < ((j == 7) ? 9 : 11);
  if (qkv) { if (hf == 0) phase1<true, true, 3>(P1ARGS); else phase1<false, true, 3>(P1ARGS); }
  else if (blk2) { if (hf == 0) phase1<true, false, 3>(P1ARGS); else phase1<false, false, 3>(P1ARGS); }
  else { if (hf == 0) phase1<true, false, 2>(P1ARGS); else phase1<false, false, 2>(P1ARGS); }
  asm volatile("" :: "v"(cpd));
  b2_scalars(b2c, P.wo, P.w2, ldsb, hf, c, rt, ct);
  const __amdgpu_buffer_rsrc_t act = __builtin_amdgcn_make_buffer_rsrc(P.ws + WS_ACT, 0, ACT_BYTES, 0x00020000);
  const int tb0 = b2c.tb0;
  TS(41);
  bar();
  if (tid == 0) __hip_atomic_store((GAS u64*)(P.ws + WS_FLAG + p * 128 + (qkv ? j : 8 + j) * 8), tag, __ATOMIC_RELAXED, __HIP_MEMORY_SCOPE_AGENT);
  STAMP(5);
  TS(42);
#pragma unroll
  for (int i = 4 * NE; i < 40; i++) br_read(BR, smem, tb0, i);
  wait_lgkm0();
  bar();
#pragma unroll
  for (int s = 18 + NE; s < 30; s++) b2_issue(b2c, s);
  const int orow = rt * 64 + hf * 32 + (lane & 31), ocol = ct * 64 + c * 16 + (lane >> 5) * 4;
  const u32x2 xs0 = *(const GAS u32x2*)(P.x + orow * 1024 + ocol * 2), xs1 = *(const GAS u32x2*)(P.x + orow * 1024 + ocol * 2 + 16);
  const u32x2 bv0 = *(const GAS u32x2*)(P.b2 + ocol * 2), bv1 = *(const GAS u32x2*)(P.b2 + ocol * 2 + 16);
  TS(45);
  if (wave == 0) {
    if (b == 0) {
      const GAS u64* fa = (const GAS u64*)(P.ws + WS_FLAG) + lane;
#pragma unroll 1
      for (unsigned spin = 0; spin < SPIN_CAP; spin++) {
        u64 v0 = __hip_atomic_load(fa, __ATOMIC_RELAXED, __HIP_MEMORY_SCOPE_AGENT), v1 = __hip_atomic_load(fa + 64, __ATOMIC_RELAXED, __HIP_MEMORY_SCOPE_AGENT);
        u64 v2 = __hip_atomic_load(fa + 128, __ATOMIC_RELAXED, __HIP_MEMORY_SCOPE_AGENT), v3 = __hip_atomic_load(fa + 192, __ATOMIC_RELAXED, __HIP_MEMORY_SCOPE_AGENT);
        if (__all(v0 == tag && v1 == tag && v2 == tag && v3 == tag)) break;
        __builtin_amdgcn_s_sleep(1);
      }
      if (lane == 0) __hip_atomic_store((GAS u64*)P.ws, tag, __ATOMIC_RELAXED, __HIP_MEMORY_SCOPE_AGENT);
    } else {
      const GAS u64* fl = (const GAS u64*)(P.ws + WS_FLAG + (rt >> 1) * 128 + (lane & 15) * 8);
      if (POLL == 2) {
        u64 v0 = __hip_atomic_load(fl, __ATOMIC_RELAXED, __HIP_MEMORY_SCOPE_AGENT);
#pragma unroll 1
        for (unsigned spin = 0; spin < SPIN_CAP; spin++) {
          __builtin_amdgcn_s_sleep(2);
          u64 v1 = __hip_atomic_load(fl, __ATOMIC_RELAXED, __HIP_MEMORY_SCOPE_AGENT);
          if (__all(v0 == tag)) break;
          __builtin_amdgcn_s_sleep(2);
          v0 = __hip_atomic_load(fl, __ATOMIC_RELAXED, __HIP_MEMORY_SCOPE_AGENT);
          if (__all(v1 == tag)) break;
        }
      } else {
#pragma unroll 1
        for (unsigned spin = 0; spin < SPIN_CAP; spin++) {
          if (__all(__hip_atomic_load(fl, __ATOMIC_RELAXED, __HIP_MEMORY_SCOPE_AGENT) == tag)) break;
          if (POLL == 0) __builtin_amdgcn_s_sleep(1);
        }
      }
    }
  }
  TS(49);
  bar();
  TS(50);
  STAMP(6);
  const int abase = (rt * 2 + hf) * NK16;
  auto aload = [&](int s) -> u32x4 {
    const int vo = (s == 29 && b2c.nf == 21) ? 0x7f000000 : lane * 16;
    return __builtin_amdgcn_raw_buffer_load_b128(act, vo, (abase + phys_k16(b2c, s)) * 1024, 16);
  };
  u32x4 AF[PF];
  f32x16 o2[2];
#pragma unroll
  for (int e = 0; e < 16; e++) { o2[0][e] = 0.f; o2[1][e] = 0.f; }
#pragma unroll
  for (int s = 0; s < PF; s++) AF[s] = aload(s);
#pragma unroll
  for (int s = 0; s < 30; s++) {
    if (s == 12 && !DSIDE) { wait_lgkm0(); bar(); }
    if (P2PRIO == 1 && s == 18 && hf == 1) __builtin_amdgcn_s_setprio(1);
    if (P2PRIO == 2 && s == 22 && hf == 1) __builtin_amdgcn_s_setprio(1);
    if (s == 18) { wait_lgkm0(); bar(); }
    u32x4 B0, B1;
    if (s < 10) { B0 = (u32x4){BR.lo[s][0].x, BR.lo[s][0].y, BR.hi[s][0].x, BR.hi[s][0].y}; B1 = (u32x4){BR.lo[s][1].x, BR.lo[s][1].y, BR.hi[s][1].x, BR.hi[s][1].y}; }
    else {
      u32x2 lo = trd(smem + b2grp(s) * 8192 + tb0), hi = trd(smem + b2grp(s) * 8192 + tb0 + 512);
      B0 = (u32x4){lo.x, lo.y, hi.x, hi.y};
      lo = trd(smem + b2grp(s) * 8192 + (tb0 ^ 64)); hi = trd(smem + b2grp(s) * 8192 + (tb0 ^ 64) + 512);
      B1 = (u32x4){lo.x, lo.y, hi.x, hi.y};
    }
    const u32x4 a = AF[s % PF];
    o2[0] = __builtin_amdgcn_mfma_f32_32x32x16_bf16(b8(B0), b8(a), o2[0], 0, 0, 0);
    o2[1] = __builtin_amdgcn_mfma_f32_32x32x16_bf16(b8(B1), b8(a), o2[1], 0, 0, 0);
    if (s + PF < 30) AF[s % PF] = aload(s + PF);
    SB();
    if (s == 0) TS(51);
    if (s == 9) TS(53);
    if (s == 19) TS(55);
  }
  STAMP(7);
  TS(57);
  constexpr int XCH = 3 * 8192;
#pragma unroll
  for (int lb = 1; lb < 4; lb++)
#pragma unroll
    for (int hh = 0; hh < 2; hh++) {
      const int r0 = (lb & 1) * 8 + hh * 4;
      *(LAS f32x4*)(smem + XCH + ((hf * 4 + ((c + lb) & 3)) * 3 + lb - 1) * 2048 + hh * 1024 + lane * 16) =
          (f32x4){o2[lb >> 1][r0], o2[lb >> 1][r0 + 1], o2[lb >> 1][r0 + 2], o2[lb >> 1][r0 + 3]};
    }
  f32x4 u0 = {o2[0][0] + (bf_lo(xs0.x) + bf_lo(bv0.x)), o2[0][1] + (bf_hi(xs0.x) + bf_hi(bv0.x)), o2[0][2] + (bf_lo(xs0.y) + bf_lo(bv0.y)), o2[0][3] + (bf_hi(xs0.y) + bf_hi(bv0.y))};
  f32x4 u1 = {o2[0][4] + (bf_lo(xs1.x) + bf_lo(bv1.x)), o2[0][5] + (bf_hi(xs1.x) + bf_hi(bv1.x)), o2[0][6] + (bf_lo(xs1.y) + bf_lo(bv1.y)), o2[0][7] + (bf_hi(xs1.y) + bf_hi(bv1.y))};
  wait_lgkm0();
  TS(59);
  bar();
  TS(60);
#pragma unroll
  for (int i = 0; i < 3; i++) { u0 += *(const LAS f32x4*)(smem + XCH + (wave * 3 + i) * 2048 + lane * 16); u1 += *(const LAS f32x4*)(smem + XCH + (wave * 3 + i) * 2048 + 1024 + lane * 16); }
  {
    auto s0 = __builtin_amdgcn_permlane32_swap(pk_bf16(u0[0], u0[1]), pk_bf16(u1[0], u1[1]), false, false);
    auto s1 = __builtin_amdgcn_permlane32_swap(pk_bf16(u0[2], u0[3]), pk_bf16(u1[2], u1[3]), false, false);
    if (OUTSC == 0) *(GAS u32x4*)(P.out + orow * 1024 + (ct * 64 + c * 16 + (lane >> 5) * 8) * 2) = (u32x4){s0[0], s1[0], s0[1], s1[1]};
    else { const __amdgpu_buffer_rsrc_t ors = __builtin_amdgcn_make_buffer_rsrc(P.out, 0, 2048 * 1024, 0x00020000);
      __builtin_amdgcn_raw_buffer_store_b128((u32x4){s0[0], s1[0], s0[1], s1[1]}, ors, orow * 1024 + (ct * 64 + c * 16 + (lane >> 5) * 8) * 2, 0, OUTSC); }
  }
  STAMP(8);
  TS(61);
}

extern "C" void kernel_launch(void* const* d_in, const int* in_sizes, int n_in, void* d_out, int out_size, void* d_ws, size_t ws_size,
                              hipStream_t stream) {
  Params P;
  P.x = (const char*)d_in[0]; P.wqkv = (const char*)d_in[1]; P.wo = (const char*)d_in[2];
  P.w1 = (const char*)d_in[4]; P.b1 = (const char*)d_in[5]; P.w2 = (const char*)d_in[6]; P.b2 = (const char*)d_in[7];
  P.out = (char*)d_out; P.ws = (char*)d_ws;
  fused_block<<<256, 512, 0, stream>>>(P);
}
```

```cpp
#include <hip/hip_runtime.h>
#include <stdint.h>

typedef __attribute__((ext_vector_type(8))) __bf16 bf16x8;
typedef __attribute__((ext_vector_type(2))) __bf16 bf16x2;
typedef __attribute__((ext_vector_type(4))) short s16x4;
typedef __attribute__((ext_vector_type(4))) float f32x4;
typedef __attribute__((ext_vector_type(2))) float f32x2;
typedef __attribute__((ext_vector_type(16))) float f32x16;
typedef __attribute__((ext_vector_type(4))) unsigned u32x4;
typedef __attribute__((ext_vector_type(2))) unsigned u32x2;
#define LAS __attribute__((address_space(3)))
#define GAS __attribute__((address_space(1)))
typedef unsigned long long u64;

constexpr int PF = 4;
constexpr int NE = 4;
constexpr int BG = 3;
constexpr int NK16 = 118;
constexpr int SLOT = 43008, XB = 18432;
constexpr int ST_MEAN = 129024, ST_CS = ST_MEAN + 1152, PAIRF = ST_MEAN + 1920, KVX = 19 * 8192;
constexpr int WS_FLAG = 1024, WS_STAT = 65536, WS_ACT = 131072;
constexpr int ACT_BYTES = 64 * NK16 * 1024;
constexpr int CODE_LINES = 428;
constexpr unsigned SPIN_CAP = 1u << 20;
constexpr unsigned ONES = 0x3f803f80u;

struct Params {
  const char* x; const char* wqkv; const char* wo; const char* w1; const char* b1; const char* w2; const char* b2;
  char* out; char* ws;
};

#define SB() __builtin_amdgcn_sched_barrier(0)
__device__ __forceinline__ void set_m0(unsigned base, int add) { asm volatile("s_add_i32 m0, %0, %1" :: "s"(base), "i"(add) : "memory", "scc"); }
__device__ __forceinline__ void nop1() { asm volatile("s_nop 0"); }
template <int IMM> __device__ __forceinline__ void glds(unsigned voff, const char* sbase) {
  asm volatile("global_load_lds_dwordx4 %0, %1 offset:%2" :: "v"(voff), "s"(sbase), "i"(IMM) : "memory");
}
__device__ __forceinline__ void glds_mask(unsigned voff, const char* sbase, u64 mask) {
  asm volatile("s_mov_b64 exec, %2\n\tglobal_load_lds_dwordx4 %0, %1\n\ts_mov_b64 exec, -1" :: "v"(voff), "s"(sbase), "s"(mask) : "memory");
}
constexpr int JUNK = 17 * 8192;
__device__ __forceinline__ void touch(unsigned voff, const void* sbase, u64 mask, unsigned ldsb) {
  asm volatile("s_add_i32 m0, %3, %4\n\ts_mov_b64 exec, %2\n\tglobal_load_lds_dword %0, %1\n\ts_mov_b64 exec, -1" :: "v"(voff), "s"(sbase), "s"(mask), "s"(ldsb), "i"(JUNK) : "memory", "scc");
}
template <int N> __device__ __forceinline__ void wait_vm() { asm volatile("s_waitcnt vmcnt(%0) lgkmcnt(0)" :: "i"(N) : "memory"); }
__device__ __forceinline__ void wait_lgkm0() { asm volatile("s_waitcnt lgkmcnt(0)" ::: "memory"); }
__device__ __forceinline__ void bar() { asm volatile("s_barrier" ::: "memory"); }
__device__ __forceinline__ unsigned pk_bf16(float a, float b) { f32x2 p = {a, b}; return __builtin_bit_cast(unsigned, __builtin_convertvector(p, bf16x2)); }
__device__ __forceinline__ u32x2 trd(const LAS char* p) { return __builtin_bit_cast(u32x2, __builtin_amdgcn_ds_read_tr16_b64_v4i16((LAS s16x4*)p)); }
__device__ __forceinline__ bf16x8 b8(u32x4 v) { return __builtin_bit_cast(bf16x8, v); }
__device__ __forceinline__ float bf_lo(unsigned v) { return __uint_as_float(v << 16); }
__device__ __forceinline__ float bf_hi(unsigned v) { return __uint_as_float(v & 0xffff0000u); }
__device__ __forceinline__ float sel0(float v, u64 m) { float r; asm volatile("v_cndmask_b32 %0, %1, 0, %2" : "=v"(r) : "v"(v), "s"(m)); return r; }
__device__ __forceinline__ float max3f(float a, float b, float c) { return fmaxf(fmaxf(a, b), c); }
__device__ __forceinline__ u64 mix64(u64 z) {
  z += 0x9E3779B97F4A7C15ull; z = (z ^ (z >> 30)) * 0xBF58476D1CE4E5B9ull; z = (z ^ (z >> 27)) * 0x94D049BB133111EBull; return z ^ (z >> 31);
}
template <int CTRL> __device__ __forceinline__ float dpp_add(float v) {
  return v + __builtin_bit_cast(float, __builtin_amdgcn_update_dpp(0, __builtin_bit_cast(int, v), CTRL, 0xf, 0xf, true));
}
__device__ __forceinline__ float half_sum(float v) {
  v = dpp_add<0xB1>(v); v = dpp_add<0x4E>(v); v = dpp_add<0x141>(v); v = dpp_add<0x140>(v);
  auto a = __builtin_amdgcn_permlane16_swap(__float_as_uint(v), __float_as_uint(v), false, false);
  return __uint_as_float(a[0]) + __uint_as_float(a[1]);
}
#define LDSP(T, a) ((LAS T*)(size_t)(a))

__device__ __forceinline__ constexpr int b2grp(int s) {
  return s < 3 ? 16 + s : s < 7 ? 8 + s : s < 18 ? s - 7 : s < 21 ? s - 2 : s < 25 ? s - 10 : s < 28 ? s - 25 : s == 28 ? 15 : 19;
}
struct B2Ctx { const char* wo; const char* w2; int a0, rota, f0, nf, rotf; unsigned voff, vhalf, mhalf, mboth, vq[4], vr[4]; int tb0; };
__device__ __forceinline__ void b2_scalars(B2Ctx& b, const char* wo, const char* w2, unsigned ldsb, int hf, int c, int rt, int ct) {
  b.wo = wo + ct * 128; b.w2 = w2 + ct * 128; b.mboth = ldsb + c * 2048; b.mhalf = b.mboth + hf * 1024;
  b.a0 = 8 * c; b.f0 = c < 3 ? 22 * c : 65; b.nf = c < 2 ? 22 : 21;
  b.rota = ((ct + rt) & 1) * 4; b.rotf = ((ct + rt) & 1) * 10;
}
__device__ __forceinline__ int phys_k16(const B2Ctx& c, int s) {
  if (s < 8) return c.a0 + (s ^ c.rota);
  int t = s - 8 + c.rotf; t = t >= c.nf ? t - c.nf : t;
  return 32 + c.f0 + t;
}
__device__ __forceinline__ const char* b2_src(const B2Ctx& c, int s) {
  return s < 8 ? c.wo + phys_k16(c, s) * 16384 : c.w2 + (phys_k16(c, s) - 32) * 16384;
}
__device__ __forceinline__ void b2_issue(const B2Ctx& c, int s) {
  const char* src = b2_src(c, s);
  set_m0(c.mhalf, b2grp(s) * 8192); nop1();
  glds<0>(c.vhalf, src);
}
__device__ __forceinline__ void b2_issue2(const B2Ctx& c, int s) {
  const char* src = b2_src(c, s);
  set_m0(c.mboth, b2grp(s) * 8192); nop1();
  glds<0>(c.vq[0], src); glds<1024>(c.vr[0], src);
}
struct BRegs { u32x2 lo[10][2], hi[10][2]; };
__device__ __forceinline__ void br_read(BRegs& BR, const LAS char* smem, int tb0, int i) {
  const int s = i >> 2, cb = (i >> 1) & 1;
  if (i & 1) BR.hi[s][cb] = trd(smem + b2grp(s) * 8192 + (tb0 ^ (cb * 64)) + 512); else BR.lo[s][cb] = trd(smem + b2grp(s) * 8192 + (tb0 ^ (cb * 64)));
}

struct Ld { unsigned vo[6]; const char* base0; unsigned mbase; };
struct Epi { f32x4 mp, mc, mp1; unsigned selA, selB; float NEG; float mu[5], rs[5], csv; f32x4 mu4[5], rs4[5], cs[3]; u32x2 b1v[3]; };

template <bool H0, bool QKV, int NS>
struct P1 {
  static constexpr int NRB = H0 ? 5 : 4;
  static constexpr int NL = H0 ? (QKV ? 5 : 4) : 6;
  static constexpr int KSTRIDE = H0 ? 128 : QKV ? 64 * 3072 : 64 * 2752;
  struct Frag { u32x4 A[NRB]; u32x2 Bl[3], Bh[3]; };
  Ld ld; int hdelta; unsigned mbaseh; u64 hmask;
  const char* cb;
  unsigned pb, pa0, pa1;
  u32x4 ones;

  __device__ __forceinline__ void open(int ch) { cb = ld.base0 + ch * KSTRIDE; set_m0(ld.mbase, (ch % 3) * SLOT); }
  template <int I> __device__ __forceinline__ void ld1(int ch) const {
    if (I >= NL) return;
    if (H0 && I == 4) { set_m0(mbaseh, (ch % 3) * SLOT); glds_mask(ld.vo[4], cb + hdelta, hmask); return; }
    glds<(H0 ? I * 1024 : I * 1024 - 3072)>(ld.vo[I], cb);
  }
  __device__ __forceinline__ void load1(int ch, int i) {
    if (i == 0) { open(ch); nop1(); ld1<0>(ch); } else if (i == 1) ld1<1>(ch); else if (i == 2) ld1<2>(ch);
    else if (i == 3) ld1<3>(ch); else if (i == 4) ld1<4>(ch); else ld1<5>(ch);
  }
  __device__ __forceinline__ void rd(Frag& f, int ks, int n) const {
    constexpr int ord[11] = {0, 1, 6, 2, 3, 4, 5, 7, 8, 9, 10};
    if (n >= 11) return;
    const int i = ord[n];
    if (i < 6) {
      const int s = i >> 1;
      if (s >= NS) return;
      if (i & 1) f.Bh[s] = trd(LDSP(const char, pb) + (32 * ks + 4) * 384 + s * 128); else f.Bl[s] = trd(LDSP(const char, pb) + (32 * ks) * 384 + s * 128);
    } else if (i - 6 < NRB) {
      const int r = H0 ? (i - 5) % 5 : i - 6;
      if (!(H0 && !QKV && r == 0)) f.A[r] = *(const LAS u32x4*)(LDSP(const char, ks ? pa1 : pa0) + r * 2048);
    }
  }
  __device__ __forceinline__ void advance(int delta) {
    asm volatile("v_add_u32 %0, %3, %0\n\tv_add_u32 %1, %3, %1\n\tv_add_u32 %2, %3, %2" : "+v"(pb), "+v"(pa0), "+v"(pa1) : "i"(delta));
  }
  template <bool LAST = false, class F> __device__ __forceinline__ void mma(f32x4 (&acc)[5][3], Frag& f, F&& fill) const {
#pragma unroll
    for (int rr = 0; rr < NRB; rr++) {
      const int r = !H0 ? rr : LAST ? (rr + 4) % 5 : (rr + 1) % 5;
#pragma unroll
      for (int s = 0; s < 3; s++) {
        if (s < NS) {
          const u32x4 B = {f.Bl[s].x, f.Bl[s].y, f.Bh[s].x, f.Bh[s].y};
          const u32x4 A = (H0 && !QKV && r == 0) ? ones : f.A[r];
          if (QKV && s == 2) acc[r][s] = __builtin_amdgcn_mfma_f32_16x16x32_bf16(b8(A), b8(B), acc[r][s], 0, 0, 0);
          else acc[r][s] = __builtin_amdgcn_mfma_f32_16x16x32_bf16(b8(B), b8(A), acc[r][s], 0, 0, 0);
          SB();
        }
        fill(3 * rr + s);
        SB();
      }
    }
  }
  template <class F> __device__ __forceinline__ void stepA(f32x4 (&acc)[5][3], Frag& cur, Frag& nx, int delta, F&& site) {
    mma(acc, cur, [&](int n) {
      rd(nx, 1, n);
      constexpr int S0 = H0 ? 4 : 2;
      if (n >= S0 && (n - S0) % 4 == 0 && (n - S0) / 4 < 3) site((n - S0) / 4);
      if (n == 3 * NRB - 2 && delta != 0) advance(delta);
    });
  }
  template <class S, class F> __device__ __forceinline__ void stepB(f32x4 (&acc)[5][3], Frag& cur, Frag& nx, S&& sync, F&& site) const {
    mma(acc, cur, [&](int n) {
      if (n == BG - 1) sync();
      if (n >= BG - 1) { const int i = n - (BG - 1); if (i < 2) { rd(nx, 0, 2 * i); rd(nx, 0, 2 * i + 1); } else rd(nx, 0, i + 2); }
      constexpr int S0 = H0 ? 6 : 5, SD = H0 ? 4 : 3;
      if (n >= S0 && (n - S0) % SD == 0 && (n - S0) / SD < 3) site((n - S0) / SD);
    });
  }
};

template <bool H0, bool QKV, int NSB>
__device__ __forceinline__ void phase1(const Params& P, LAS char* smem, unsigned ldsb, const Ld& ld, int p, int j, int c, int rt, int ct,
                                       B2Ctx& b2c, BRegs& BR, u64& tag) {
  typedef P1<H0, QKV, NSB> T;
  constexpr int NL = T::NL, HF = H0 ? 0 : 1;
  const int lane = threadIdx.x & 63;
  f32x4 acc[5][3]; Epi ep;
  T t;
  t.ld = ld;
  t.mbaseh = ldsb + 4 * c * 128; t.hdelta = p > 0 ? -16384 : 0; t.hmask = c == 0 ? 0xffff0000ull : 0xffffffffull;
  u64 e_old;
  {
    const int g = lane >> 4, m = lane & 15, q4 = m >> 2, pp = lane & 3;
    t.pa0 = ldsb + (H0 ? 0 : 5 * 2048) + m * 128 + ((g ^ (m >> 1)) * 16); t.pa1 = t.pa0 ^ 64;
    t.pb = ldsb + XB + (8 * g + q4) * 384 + (((2 * c + (pp >> 1)) ^ (2 * ((q4 >> 1) | ((g & 1) << 1)))) * 16) + (pp & 1) * 8;
    const unsigned o = m == 0 ? ONES : 0u; t.ones = (u32x4){o, o, o, o};
    if (H0 && !QKV) asm volatile("" : "+v"(t.ones));
    if (H0 && QKV) { if (c == 0 && lane < 48) *(LAS u32x4*)(smem + g * SLOT + m * 16) = (u32x4){ONES, ONES, ONES, ONES}; }
  }
#pragma unroll
  for (int r = 0; r < 5; r++)
#pragma unroll
    for (int s = 0; s < 3; s++) acc[r][s] = (f32x4){0.f, 0.f, 0.f, 0.f};
  typename T::Frag F0, F1;
  const int srow = p * 128 + ((QKV ? 0 : 8) + j) * 8 + 2 * c;
  auto row_stats = [&]() {
    const LAS char* q = smem + 16 * 8192 + c * 2048 + (lane >> 5) * 1024 + (lane & 31) * 16;
    const u32x4 x0 = *(const LAS u32x4*)q, x1 = *(const LAS u32x4*)(q + 512);
    f32x4 v[4] = {{bf_lo(x0.x), bf_hi(x0.x), bf_lo(x0.y), bf_hi(x0.y)}, {bf_lo(x0.z), bf_hi(x0.z), bf_lo(x0.w), bf_hi(x0.w)},
                  {bf_lo(x1.x), bf_hi(x1.x), bf_lo(x1.y), bf_hi(x1.y)}, {bf_lo(x1.z), bf_hi(x1.z), bf_lo(x1.w), bf_hi(x1.w)}};
    const f32x4 s4 = (v[0] + v[1]) + (v[2] + v[3]);
    const float mean = half_sum((s4[0] + s4[1]) + (s4[2] + s4[3])) * (1.f / 512.f);
#pragma unroll
    for (int e = 0; e < 4; e++) v[e] = v[e] - mean;
    const f32x4 q4 = (v[0] * v[0] + v[1] * v[1]) + (v[2] * v[2] + v[3] * v[3]);
    const float rs = __builtin_amdgcn_rsqf(half_sum((q4[0] + q4[1]) + (q4[2] + q4[3])) * (1.f / 512.f) + 1e-5f);
    const u32x2 gr = {__float_as_uint((lane & 1) ? rs : mean), (unsigned)tag};
    asm volatile("s_mov_b64 exec, %3\n\tglobal_store_dwordx2 %0, %1, %2 sc1\n\ts_mov_b64 exec, -1"
                 :: "v"((srow + (lane >> 5)) * 16 + (lane & 1) * 8), "v"(gr), "s"(P.ws + WS_STAT), "s"(0x0000000300000003ull) : "memory");
  };
  asm volatile("s_load_dwordx2 %0, %1, 0x0" : "=s"(e_old) : "s"(P.ws) : "memory");
  unsigned pfo; const char* pfw;
  auto warm = [&](int k) { touch(pfo, pfw, 0x3ull << (2 * k), ldsb); };
  unsigned wpo; const char* wpw;
  constexpr int NW = H0 ? 1 : 0;
  auto w_warm = [&]() { touch(wpo, wpw, 0x7fffffull, ldsb); };
  if (NW) {
    const int w0 = blockIdx.x * 91 + c * 23, wl = c == 3 ? 21 : 22, wn = 12288 - w0;
    wpw = w0 < 12288 ? P.wqkv + w0 * 128 : P.w1 + (w0 - 12288) * 128;
    const int wt = (w0 < 12288 && wn - 1 < wl) ? wn - 1 : wl;
    wpo = (lane < wt ? lane : wt) * 128; asm volatile("" : "+v"(wpo), "+s"(wpw));
  }
  unsigned sto, stw, str1, str4, csw, csw1, kvw, vq[4], vr[4];
  const char *bA, *bB, *bC;
  auto later = [&](int k) {
    const int g = lane >> 4, m = lane & 15, w = 4 * HF + c;
    if (k == 0) {
      u64 z = mix64(e_old); unsigned lo = __builtin_amdgcn_readfirstlane((unsigned)z), hi = __builtin_amdgcn_readfirstlane((unsigned)(z >> 32));
      asm volatile("" : "+s"(lo), "+s"(hi)); tag = ((u64)hi << 32) | lo;
    }
    if (k == 1) {
      const int l0 = blockIdx.x * 59 + w * 8, lim = blockIdx.x * 59 + 58 - l0;
      const int n0 = 4096 - l0;
      pfw = l0 < 4096 ? P.wo + l0 * 128 : P.w2 + (l0 - 4096) * 128;
      const int top = (l0 < 4096 && n0 - 1 < lim) ? n0 - 1 : lim;
      const int l = lane & 7;
      pfo = (l < top ? l : top) * 128; asm volatile("" : "+v"(pfo), "+s"(pfw));
      const int sti = 18 * w + (lane < 18 ? lane : 17), row = p * 128 - 16 + sti;
      sto = (row < 0 ? 0 : row) * 16; stw = ldsb + ST_MEAN + sti * 4; str1 = ldsb + ST_MEAN + (64 * HF + m) * 4;
      str4 = QKV ? ldsb + ST_MEAN + (64 * HF + 4 * g) * 4 : ldsb + KVX + (c * 16 + 4 * g) * 2;
      csw = ldsb + ST_CS + (c * 16 + 4 * g) * 4; csw1 = ldsb + ST_CS + (128 + c * 16 + m) * 4; kvw = ldsb + KVX + c * 2048 + lane * 16;
      asm volatile("" : "+v"(sto), "+v"(stw), "+v"(str1), "+v"(str4), "+v"(csw));
      if (QKV) asm volatile("" : "+v"(csw1), "+v"(kvw));
      b2c.voff = (lane >> 3) * 1024 + (((lane & 7) ^ ((g & 1) << 2)) * 16);
#pragma unroll
      for (int i = 0; i < 4; i++) vq[i] = b2c.voff + i * 16384;
      b2c.vhalf = b2c.voff + HF * 8192;
      b2c.tb0 = c * 2048 + (8 * (g >> 1) + (m >> 2)) * 128 + (((2 * (g & 1) + ((lane & 3) >> 1)) ^ ((m >> 3) << 2)) * 16) + (lane & 1) * 8;
      asm volatile("" : "+v"(b2c.vhalf), "+v"(b2c.tb0));
      if (H0) {
#pragma unroll
        for (int i = 0; i < 4; i++) { vr[i] = vq[i] + 7168; asm volatile("" : "+v"(vq[i]), "+v"(vr[i])); b2c.vq[i] = vq[i]; b2c.vr[i] = vr[i]; }
      }
      if (QKV) {
        constexpr u64 MP[4] = {0x555005500050000ull, 0xaaa00aa000a0000ull, 0x1555015500150001ull, 0x2aaa02aa002a0002ull}, MC[4] = {0x5000550055505555ull, 0xa000aa00aaa0aaaaull, 0x4000540055405554ull, 0x8000a800aa80aaa8ull};
        ep.NEG = -3.0e38f; asm volatile("" : "+v"(ep.NEG));
        const u64 live = (H0 && p == 0) ? 0ull : ~0ull;
#pragma unroll
        for (int e = 0; e < 4; e++) {
          ep.mp[e] = sel0(ep.NEG, MP[e]); ep.mc[e] = sel0(ep.NEG, MC[e]);
          ep.mp1[e] = H0 ? sel0(ep.NEG, MP[e] & live) : ep.mp[e];
        }
        ep.selA = (m & 1) ? 0x01000c0cu : 0x0c0c0100u; ep.selB = ep.selA + ((m & 1) ? 0x02020000u : 0x00000202u);
        asm volatile("" : "+v"(ep.selA), "+v"(ep.selB));
      }
    }
    if (k == 2 && H0) {
      b2_scalars(b2c, P.wo, P.w2, ldsb, HF, c, rt, ct);
      bA = b2_src(b2c, 0); asm volatile("" : "+s"(bA), "+s"(b2c.mboth));
    }
    if (k == 3 && H0) { bB = b2_src(b2c, 4); asm volatile("" : "+s"(bB)); }
    if (k == 4 && H0) { bC = b2_src(b2c, 8); asm volatile("" : "+s"(bC)); }
    if (k == 5 && H0) { if (lane == 0) *LDSP(unsigned, ldsb + PAIRF + c * 4) = 0u; }
  };
  auto b2_early = [&](int i) {
    if (H0) { const char* src = i < 4 ? bA : i < 8 ? bB : bC; set_m0(b2c.mboth, b2grp(i) * 8192); nop1(); glds<0>(vq[i & 3], src); glds<1024>(vr[i & 3], src); }
  };
  later(1);
  SB();
#pragma unroll
  for (int i = 0; i < 6; i++) t.load1(1, i);
  asm volatile("s_waitcnt lgkmcnt(0)" : "+s"(e_old) :: "memory");
  later(0);
  if (H0) {
    wait_vm<2 * NL>();
    row_stats();
  }
  SB();
  wait_vm<NL + NW>();
  if (H0) w_warm();
  bar();
#pragma unroll
  for (int n = 0; n < 11; n++) t.rd(F0, 0, n);
  u32x4 st;
  t.stepA(acc, F0, F1, SLOT, [&](int k) { t.load1(2, 2 * k); t.load1(2, 2 * k + 1); });
  t.stepB(acc, F1, F0, [&]() { wait_vm<NL + 2 * NW>(); bar(); }, [&](int k) { t.load1(3, k); });
#pragma unroll
  for (int ch = 1; ch < 5; ch++) {
    t.stepA(acc, F0, F1, ch % 3 == 2 ? -2 * SLOT : SLOT, [&](int k) { t.load1(ch + 2, 3 + k); if (k == 2) warm(ch - 1); });
    t.stepB(acc, F1, F0, [&]() {
      if (ch == 1) wait_vm<NL + 1>(); else wait_vm<NL + 2>();
      bar();
    }, [&](int k) { t.load1(ch + 3, k); if (k == 1) later(1 + ch); });
  }
  auto stats_to_lds = [&]() {
    const unsigned tg = (unsigned)tag;
    if (!__all(st.y == tg && st.w == tg)) {
      const GAS u64* sp = (const GAS u64*)(P.ws + WS_STAT + sto);
#pragma unroll 1
      for (unsigned spin = 0; spin < SPIN_CAP; spin++) {
        u64 a = __hip_atomic_load(sp, __ATOMIC_RELAXED, __HIP_MEMORY_SCOPE_AGENT), b = __hip_atomic_load(sp + 1, __ATOMIC_RELAXED, __HIP_MEMORY_SCOPE_AGENT);
        st = (u32x4){(unsigned)a, (unsigned)(a >> 32), (unsigned)b, (unsigned)(b >> 32)};
        if (__all(st.y == tg && st.w == tg)) break;
        __builtin_amdgcn_s_sleep(1);
      }
    }
    *LDSP(unsigned, stw) = st.x; *LDSP(unsigned, stw + 576) = st.z;
  };
  auto row_ops = [&](int r) {
    ep.mu[r] = *LDSP(const float, str1 + 64 * r); ep.rs[r] = *LDSP(const float, str1 + 64 * r + 576);
    if (QKV) { ep.mu4[r] = *LDSP(const f32x4, str4 + 64 * r); ep.rs4[r] = *LDSP(const f32x4, str4 + 64 * r + 576); }
    else if (r < 4) ep.b1v[r - 1] = *LDSP(const u32x2, str4 + 128 * (r - 1));
  };
  t.stepA(acc, F0, F1, -2 * SLOT, [&](int k) { t.load1(7, 3 + k); });
  t.stepB(acc, F1, F0, [&]() {
    wait_vm<NL>(); bar();
    asm volatile("global_load_dwordx4 %0, %1, %2 sc1" : "=v"(st) : "v"(sto), "s"(P.ws + WS_STAT) : "memory");
  }, [&](int k) { b2_early(k); });
  t.stepA(acc, F0, F1, SLOT, [&](int k) { b2_early(3 + k); });
  t.stepB(acc, F1, F0, [&]() { wait_vm<H0 ? 13 : 1>(); bar(); }, [&](int k) { b2_early(6 + k); });
  t.stepA(acc, F0, F1, 0, [&](int k) { b2_early(9 + k); });
  t.template mma<true>(acc, F1, [&](int n) {
    if (n == 1) { asm volatile("s_waitcnt vmcnt(%1)" : "+v"(st) : "i"(H0 ? 24 : 0) : "memory"); stats_to_lds(); }
    if (H0 && QKV && n == 6) { *LDSP(f32x4, kvw) = acc[4][1]; *LDSP(f32x4, kvw + 1024) = acc[4][2]; }
    if (H0 && n == 9) {
      if (QKV) { if (lane < 16) *LDSP(float, csw1) = acc[0][2][0]; }
      if ((lane & 15) == 0) {
        *LDSP(f32x4, csw) = acc[0][0];
        *LDSP(f32x4, csw + 256) = acc[0][1];
        if (!QKV) *LDSP(f32x4, csw + 512) = acc[0][2];
      }
    }
  });
  if (!H0) {
#pragma unroll
    for (int r = 4; r >= 1; r--)
#pragma unroll
      for (int s = 0; s < 3; s++) acc[r][s] = acc[r - 1][s];
  }
  wait_vm<H0 ? 24 - 2 * NE : 0>();
  if (H0) __builtin_amdgcn_s_setprio(2); else __builtin_amdgcn_s_setprio(1);
  bar();
  ep.cs[0] = *LDSP(const f32x4, csw); ep.cs[1] = *LDSP(const f32x4, csw + 256);
  if (QKV) ep.csv = *LDSP(const float, csw1); else ep.cs[2] = *LDSP(const f32x4, csw + 512);
  if (!H0 && QKV) { acc[0][1] = *LDSP(const f32x4, kvw); acc[0][2] = *LDSP(const f32x4, kvw + 1024); }
#pragma unroll
  for (int r = QKV ? 0 : 1; r < 5; r++) row_ops(r);
  SB();
  constexpr int hf = HF; constexpr bool qkv = QKV;
  const __amdgpu_buffer_rsrc_t act = __builtin_amdgcn_make_buffer_rsrc(P.ws + WS_ACT, 0, ACT_BYTES, 0x00020000);
  const int rt32 = p * 4 + hf * 2, tb0 = b2c.tb0;
  const char* bD = bC + 65536;
  auto side = [&](int u) {
    SB();
    if (u > 5) return;
    if (hf == 0) {
      const char* src = u < 4 ? bD : bD + 65536;
      set_m0(b2c.mboth, b2grp(12 + u) * 8192); nop1(); glds<0>(b2c.vq[u & 3], src); glds<1024>(b2c.vr[u & 3], src);
    } else {
      if (u < 4) {
#pragma unroll
        for (int i = 0; i < NE; i++) br_read(BR, smem, tb0, NE * u + i);
      }
      if (u == 5) { wait_lgkm0(); if (lane == 0) *(LAS unsigned*)(smem + PAIRF + c * 4) = 1u; }
    }
    SB();
  };
  if (qkv) {
    u32x2 qf[5], kf[5], vf[5];
#pragma unroll
    for (int r = 0; r < 5; r++) {
      f32x4 k4 = (acc[r][1] - ep.mu[r] * ep.cs[1]) * ep.rs[r], v4 = (acc[r][2] - ep.mu4[r] * ep.csv) * ep.rs4[r];
      kf[r] = (u32x2){pk_bf16(k4[0], k4[1]), pk_bf16(k4[2], k4[3])};
      vf[r] = (u32x2){pk_bf16(v4[0], v4[1]), pk_bf16(v4[2], v4[3])};
      if (r > 0) { f32x4 q4v = (acc[r][0] - ep.mu[r] * ep.cs[0]) * ep.rs[r]; qf[r] = (u32x2){pk_bf16(q4v[0], q4v[1]), pk_bf16(q4v[2], q4v[3])}; }
      side(r);
    }
    const float SC = 0.25f * 1.4426950408889634f, NEG = ep.NEG;
    float sv[4][4], mx[4];
#pragma unroll
    for (int r = 1; r < 5; r++) {
      f32x4 sp = __builtin_amdgcn_mfma_f32_16x16x16bf16_1k(__builtin_bit_cast(s16x4, kf[r - 1]), __builtin_bit_cast(s16x4, qf[r]), r == 1 ? ep.mp1 : ep.mp, 0, 0, 0);
      f32x4 sc = __builtin_amdgcn_mfma_f32_16x16x16bf16_1k(__builtin_bit_cast(s16x4, kf[r]), __builtin_bit_cast(s16x4, qf[r]), ep.mc, 0, 0, 0);
      sv[r - 1][0] = max3f(sp[0], sp[1], NEG); sv[r - 1][1] = max3f(sp[2], sp[3], NEG); sv[r - 1][2] = max3f(sc[0], sc[1], NEG); sv[r - 1][3] = max3f(sc[2], sc[3], NEG);
      mx[r - 1] = max3f(max3f(sv[r - 1][0], sv[r - 1][1], sv[r - 1][2]), sv[r - 1][3], NEG);
    }
    side(5);
    {
      auto a = __builtin_amdgcn_permlane16_swap(__float_as_uint(mx[0]), __float_as_uint(mx[1]), false, false);
      auto b = __builtin_amdgcn_permlane16_swap(__float_as_uint(mx[2]), __float_as_uint(mx[3]), false, false);
      const float u = max3f(__uint_as_float(a[0]), __uint_as_float(a[1]), NEG), v = max3f(__uint_as_float(b[0]), __uint_as_float(b[1]), NEG);
      auto d = __builtin_amdgcn_permlane32_swap(__float_as_uint(u), __float_as_uint(v), false, false);
      const unsigned t = __float_as_uint(max3f(__uint_as_float(d[0]), __uint_as_float(d[1]), NEG) * SC);
      auto e = __builtin_amdgcn_permlane32_swap(t, t, false, false);
      auto f0 = __builtin_amdgcn_permlane16_swap(e[0], e[0], false, false);
      auto f1 = __builtin_amdgcn_permlane16_swap(e[1], e[1], false, false);
      mx[0] = __uint_as_float(f0[0]); mx[1] = __uint_as_float(f0[1]); mx[2] = __uint_as_float(f1[0]); mx[3] = __uint_as_float(f1[1]);
    }
    const u32x4 one4 = {ONES, ONES, ONES, ONES};
    const f32x4 z = {0.f, 0.f, 0.f, 0.f};
    f32x4 o4[4], l4[4];
#pragma unroll
    for (int r = 0; r < 4; r++) {
      float e[4];
#pragma unroll
      for (int i = 0; i < 4; i++) e[i] = __builtin_amdgcn_exp2f(sv[r][i] * SC - mx[r]);
      const unsigned pa = pk_bf16(e[0], e[1]), pb = pk_bf16(e[2], e[3]);
      const u32x4 pf = {__builtin_amdgcn_perm(0u, pa, ep.selA), __builtin_amdgcn_perm(0u, pa, ep.selB), __builtin_amdgcn_perm(0u, pb, ep.selA), __builtin_amdgcn_perm(0u, pb, ep.selB)};
      const u32x4 vv = {vf[r].x, vf[r].y, vf[r + 1].x, vf[r + 1].y};
      o4[r] = __builtin_amdgcn_mfma_f32_16x16x32_bf16(b8(vv), b8(pf), z, 0, 0, 0);
      l4[r] = __builtin_amdgcn_mfma_f32_16x16x32_bf16(b8(one4), b8(pf), z, 0, 0, 0);
    }
    u32x2 of[4];
#pragma unroll
    for (int r = 0; r < 4; r++) {
      const f32x4 o = o4[r] * __builtin_amdgcn_rcpf(l4[r][0]);
      of[r] = (u32x2){pk_bf16(o[0], o[1]), pk_bf16(o[2], o[3])};
    }
#pragma unroll
    for (int pr = 0; pr < 2; pr++) {
      auto s0 = __builtin_amdgcn_permlane16_swap(of[2 * pr].x, of[1 + 2 * pr].x, false, false);
      auto s1 = __builtin_amdgcn_permlane16_swap(of[2 * pr].y, of[1 + 2 * pr].y, false, false);
      __builtin_amdgcn_raw_buffer_store_b128((u32x4){s0[0], s1[0], s0[1], s1[1]}, act, lane * 16 + ((rt32 + pr) * NK16 + 4 * j + c) * 1024, 0, 16);
    }
    SB();
  } else {
    const unsigned sto0 = lane * 16 + (rt32 * NK16 + 32 + 11 * j + c) * 1024;
#pragma unroll
    for (int s = 0; s < NSB; s++) {
      const f32x4 bb = {bf_lo(ep.b1v[s].x), bf_hi(ep.b1v[s].x), bf_lo(ep.b1v[s].y), bf_hi(ep.b1v[s].y)};
      f32x4 v[4], z[4];
#pragma unroll
      for (int r = 0; r < 4; r++) v[r] = acc[r + 1][s] - ep.mu[r + 1] * ep.cs[s];
      SB();
#pragma unroll
      for (int r = 0; r < 4; r++) v[r] = v[r] * ep.rs[r + 1] + bb;
      SB();
#pragma unroll
      for (int r = 0; r < 4; r++) z[r] = v[r] * v[r];
      side(3 * s);
#pragma unroll
      for (int r = 0; r < 4; r++) z[r] = z[r] * (-0.10294324f) + (-2.3022082f);
      SB();
#pragma unroll
      for (int r = 0; r < 4; r++) z[r] = z[r] * v[r];
      SB();
#pragma unroll
      for (int r = 0; r < 4; r++) z[r] = (f32x4){__builtin_amdgcn_exp2f(z[r][0]), __builtin_amdgcn_exp2f(z[r][1]), __builtin_amdgcn_exp2f(z[r][2]), __builtin_amdgcn_exp2f(z[r][3])};
      side(3 * s + 1);
#pragma unroll
      for (int r = 0; r < 4; r++) z[r] = z[r] + 1.f;
      SB();
#pragma unroll
      for (int r = 0; r < 4; r++) z[r] = (f32x4){__builtin_amdgcn_rcpf(z[r][0]), __builtin_amdgcn_rcpf(z[r][1]), __builtin_amdgcn_rcpf(z[r][2]), __builtin_amdgcn_rcpf(z[r][3])};
      side(3 * s + 2);
#pragma unroll
      for (int r = 0; r < 4; r++) v[r] = v[r] * z[r];
      SB();
      u32x2 hfr[4];
#pragma unroll
      for (int r = 0; r < 4; r++) hfr[r] = (u32x2){pk_bf16(v[r][0], v[r][1]), pk_bf16(v[r][2], v[r][3])};
      SB();
#pragma unroll
      for (int pr = 0; pr < 2; pr++) {
        auto s0 = __builtin_amdgcn_permlane16_swap(hfr[2 * pr].x, hfr[2 * pr + 1].x, false, false);
        auto s1 = __builtin_amdgcn_permlane16_swap(hfr[2 * pr].y, hfr[2 * pr + 1].y, false, false);
        __builtin_amdgcn_raw_buffer_store_b128((u32x4){s0[0], s1[0], s0[1], s1[1]}, act, sto0 + (pr * NK16 + 4 * s) * 1024, 0, 16);
      }
      SB();
    }
  }
  if (hf == 0) {
    __builtin_amdgcn_s_setprio(0);
#pragma unroll
    for (int i = 0; i < 4 * NE; i++) br_read(BR, smem, tb0, i);
#pragma unroll 1
    for (unsigned spin = 0; spin < SPIN_CAP; spin++)
      if (__builtin_amdgcn_readfirstlane(__hip_atomic_load((LAS unsigned*)(smem + PAIRF + c * 4), __ATOMIC_RELAXED, __HIP_MEMORY_SCOPE_WORKGROUP)) == 1u) break;
    wait_vm<0>();
#pragma unroll
    for (int s = 18; s < 18 + NE; s++) b2_issue2(b2c, s);
  } else {
    wait_vm<0>();
    __builtin_amdgcn_s_setprio(0);
  }
}

typedef __attribute__((ext_vector_type(16))) unsigned u32x16;
__device__ __forceinline__ char* mkptr(unsigned lo, unsigned hi) { return (char*)(((u64)hi << 32) | lo); }
__global__ __launch_bounds__(512, 2) void fused_block(Params) {
  u32x16 ka; u32x2 kb;
  asm volatile("s_load_dwordx16 %0, %2, 0x0\n\ts_load_dwordx2 %1, %2, 0x40" : "=&s"(ka), "=&s"(kb) : "s"(__builtin_amdgcn_kernarg_segment_ptr()));
  Params P;
  auto args = [&]() {
    asm volatile("s_waitcnt lgkmcnt(0)" : "+s"(ka), "+s"(kb));
    P.x = mkptr(ka[0], ka[1]); P.wqkv = mkptr(ka[2], ka[3]); P.wo = mkptr(ka[4], ka[5]); P.w1 = mkptr(ka[6], ka[7]); P.b1 = mkptr(ka[8], ka[9]);
    P.w2 = mkptr(ka[10], ka[11]); P.b2 = mkptr(ka[12], ka[13]); P.out = mkptr(ka[14], ka[15]); P.ws = mkptr(kb[0], kb[1]);
  };
  __shared__ __attribute__((aligned(16))) char smem_[163840];
  LAS char* smem = (LAS char*)smem_;
  const unsigned ldsb = (unsigned)(size_t)smem;
  const int tid = threadIdx.x, lane = tid & 63, wave = __builtin_amdgcn_readfirstlane(tid >> 6);
  const int b = blockIdx.x, xcd = b & 7, r5 = b >> 3;
  {
    u64 pc; asm volatile("s_getpc_b64 %0" : "=s"(pc)); pc &= ~255ull;
    if (wave == 0) touch(min(lane * 32 + r5, CODE_LINES - 1) * 128, (const void*)pc, (1ull << ((CODE_LINES + 31) / 32)) - 1, ldsb);
  }
  const int hf = wave >> 2, c = wave & 3, jj = r5 >> 3;
  const bool qkv = jj < 2;
  const int p = (xcd & 1) * 8 + (r5 & 7), j = (xcd >> 1) * 2 + (jj & 1);
  const int rt = xcd * 4 + jj, ct = r5 & 7;
  Ld ld;
  const int pitch = qkv ? 3072 : 2752;
  int so0;
  if (hf == 0) {
    so0 = p * 131072 - 4096; ld.mbase = ldsb + (16 + 32 * c) * 128;
    asm volatile("" : "+s"(so0), "+s"(ld.mbase));
    const unsigned v0 = 4096 + (32 * c + (lane >> 3)) * 1024 + (((lane & 7) ^ (lane >> 4)) * 16);
    ld.vo[0] = v0; ld.vo[1] = (v0 ^ 64) + 7168; ld.vo[2] = v0 + 14336; ld.vo[3] = (v0 ^ 64) + 21504;
    { const int row = 4 * c + ((lane >> 3) & 3); ld.vo[4] = 4096 + row * 1024 + (((lane & 7) ^ (row >> 1)) * 16); }
    ld.vo[5] = (lane < (j == 7 ? 18 : 22) ? lane : 0) * 16;
  } else {
    const int secst = qkv ? 1024 : 128, lim = qkv ? 23 : (j == 7 ? 17 : 21);
    so0 = j * (qkv ? 128 : 352) + c * 16 * pitch - 4096; ld.mbase = ldsb + XB + c * 6144 + 3072;
    asm volatile("" : "+s"(so0), "+s"(ld.mbase));
#pragma unroll
    for (int r = 0; r < 3; r++) {
      const int v = lane + 64 * r, row = (v * 171) >> 12, cc = (v - 24 * row) ^ (row & 2);
      const int c0 = cc < lim ? cc : lim, c1 = (cc ^ 4) < lim ? (cc ^ 4) : lim;
      ld.vo[r] = 4096 + 3072 - 1024 * r + row * pitch + (c0 >> 3) * secst + (c0 & 7) * 16;
      ld.vo[r + 3] = 4096 - 1024 * r + (row + 8) * pitch + (c1 >> 3) * secst + (c1 & 7) * 16;
    }
  }
  unsigned xvo = lane * 16;
  asm volatile("" : "+v"(ld.vo[0]), "+v"(ld.vo[1]), "+v"(ld.vo[2]), "+v"(ld.vo[3]), "+v"(ld.vo[4]), "+v"(ld.vo[5]), "+v"(xvo));
  SB();
  args();
  ld.base0 = (hf == 0 ? P.x : qkv ? P.wqkv : P.w1) + so0;
  if (hf == 0) {
    const u64 hmask = c == 0 ? 0xffff0000ull : 0xffffffffull;
    {
      const char* xrow = P.x + (p * 128 + ((qkv ? 0 : 8) + j) * 8 + 2 * c) * 1024;
      set_m0(ldsb + c * 2048, 16 * 8192); nop1(); glds<0>(xvo, xrow); glds<1024>(xvo, xrow);
    }
    const char* cb = ld.base0;
    set_m0(ld.mbase, 0); nop1();
    glds<0>(ld.vo[0], cb); glds<1024>(ld.vo[1], cb); glds<2048>(ld.vo[2], cb); glds<3072>(ld.vo[3], cb);
    if (qkv) { set_m0(ldsb + 4 * c * 128, 0); glds_mask(ld.vo[4], cb + (p > 0 ? -16384 : 0), hmask); }
    if (!qkv && c == 0) { set_m0(ldsb, KVX); nop1(); glds<0>(ld.vo[5], P.b1 + j * 352); }
  } else {
    const char* cb = ld.base0;
    set_m0(ld.mbase, 0); nop1();
    glds<-3072>(ld.vo[0], cb); glds<-2048>(ld.vo[1], cb); glds<-1024>(ld.vo[2], cb); glds<0>(ld.vo[3], cb); glds<1024>(ld.vo[4], cb); glds<2048>(ld.vo[5], cb);

  }
  SB();
  B2Ctx b2c; BRegs BR; u64 tag;
#define P1ARGS P, smem, ldsb, ld, p, j, c, rt, ct, b2c, BR, tag
  const bool blk2 = 8 + c < ((j == 7) ? 9 : 11);
  if (qkv) { if (hf == 0) phase1<true, true, 3>(P1ARGS); else phase1<false, true, 3>(P1ARGS); }
  else if (blk2) { if (hf == 0) phase1<true, false, 3>(P1ARGS); else phase1<false, false, 3>(P1ARGS); }
  else { if (hf == 0) phase1<true, false, 2>(P1ARGS); else phase1<false, false, 2>(P1ARGS); }
  b2_scalars(b2c, P.wo, P.w2, ldsb, hf, c, rt, ct);
  const __amdgpu_buffer_rsrc_t act = __builtin_amdgcn_make_buffer_rsrc(P.ws + WS_ACT, 0, ACT_BYTES, 0x00020000);
  const int tb0 = b2c.tb0;
  bar();
  if (tid == 0) __hip_atomic_store((GAS u64*)(P.ws + WS_FLAG + p * 128 + (qkv ? j : 8 + j) * 8), tag, __ATOMIC_RELAXED, __HIP_MEMORY_SCOPE_AGENT);
#pragma unroll
  for (int i = 4 * NE; i < 40; i++) br_read(BR, smem, tb0, i);
  wait_lgkm0();
  bar();
#pragma unroll
  for (int s = 18 + NE; s < 30; s++) b2_issue(b2c, s);
  const int orow = rt * 64 + wave * 8 + (lane >> 3), ocol = ct * 64 + (lane & 7) * 8;
  u32x4 xs, bv;
  if (wave == 0) {
    if (b == 0) {
      const GAS u64* fa = (const GAS u64*)(P.ws + WS_FLAG) + lane;
#pragma unroll 1
      for (unsigned spin = 0; spin < SPIN_CAP; spin++) {
        u64 v0 = __hip_atomic_load(fa, __ATOMIC_RELAXED, __HIP_MEMORY_SCOPE_AGENT), v1 = __hip_atomic_load(fa + 64, __ATOMIC_RELAXED, __HIP_MEMORY_SCOPE_AGENT);
        u64 v2 = __hip_atomic_load(fa + 128, __ATOMIC_RELAXED, __HIP_MEMORY_SCOPE_AGENT), v3 = __hip_atomic_load(fa + 192, __ATOMIC_RELAXED, __HIP_MEMORY_SCOPE_AGENT);
        if (__all(v0 == tag && v1 == tag && v2 == tag && v3 == tag)) break;
        __builtin_amdgcn_s_sleep(1);
      }
      if (lane == 0) __hip_atomic_store((GAS u64*)P.ws, tag, __ATOMIC_RELAXED, __HIP_MEMORY_SCOPE_AGENT);
    } else {
      const GAS u64* fl = (const GAS u64*)(P.ws + WS_FLAG + (rt >> 1) * 128 + (lane & 15) * 8);
#pragma unroll 1
      for (unsigned spin = 0; spin < SPIN_CAP; spin++) {
        if (__all(__hip_atomic_load(fl, __ATOMIC_RELAXED, __HIP_MEMORY_SCOPE_AGENT) == tag)) break;
        __builtin_amdgcn_s_sleep(1);
      }
    }
  }
  bar();
  const int abase = (rt * 2 + hf) * NK16;
  auto aload = [&](int s) -> u32x4 {
    const int vo = (s == 29 && b2c.nf == 21) ? 0x7f000000 : lane * 16;
    return __builtin_amdgcn_raw_buffer_load_b128(act, vo, (abase + phys_k16(b2c, s)) * 1024, 16);
  };
  u32x4 AF[PF];
  f32x16 o2[2];
#pragma unroll
  for (int e = 0; e < 16; e++) { o2[0][e] = 0.f; o2[1][e] = 0.f; }
#pragma unroll
  for (int s = 0; s < PF; s++) AF[s] = aload(s);
  u32x2 Bl[2][2], Bh[2][2];
  auto bread = [&](int s) {
    if (s < 10 || s > 29) return;
    if (s == 18) { wait_lgkm0(); bar(); }
    const int k = s % (2);
    Bl[k][0] = trd(smem + b2grp(s) * 8192 + tb0); Bh[k][0] = trd(smem + b2grp(s) * 8192 + tb0 + 512);
    Bl[k][1] = trd(smem + b2grp(s) * 8192 + (tb0 ^ 64)); Bh[k][1] = trd(smem + b2grp(s) * 8192 + (tb0 ^ 64) + 512);
  };
#pragma unroll
  for (int s = 0; s < 30; s++) {
    const int k = s % (2);
    const u32x4 B0 = s < 10 ? (u32x4){BR.lo[s % 10][0].x, BR.lo[s % 10][0].y, BR.hi[s % 10][0].x, BR.hi[s % 10][0].y} : (u32x4){Bl[k][0].x, Bl[k][0].y, Bh[k][0].x, Bh[k][0].y};
    const u32x4 B1 = s < 10 ? (u32x4){BR.lo[s % 10][1].x, BR.lo[s % 10][1].y, BR.hi[s % 10][1].x, BR.hi[s % 10][1].y} : (u32x4){Bl[k][1].x, Bl[k][1].y, Bh[k][1].x, Bh[k][1].y};
    const u32x4 a = AF[s % PF];
    o2[0] = __builtin_amdgcn_mfma_f32_32x32x16_bf16(b8(B0), b8(a), o2[0], 0, 0, 0);
    SB();
    bread(s + 1);
    SB();
    o2[1] = __builtin_amdgcn_mfma_f32_32x32x16_bf16(b8(B1), b8(a), o2[1], 0, 0, 0);
    if (s + PF < 30) AF[s % PF] = aload(s + PF);
    if (s + PF == 30) { xs = *(const GAS u32x4*)(P.x + orow * 1024 + ocol * 2); bv = *(const GAS u32x4*)(P.b2 + ocol * 2); }
    SB();
  }
  constexpr int XCH = 3 * 8192;
  {
    const unsigned wp = XCH + c * 16384 + (hf * 32 + (lane & 31)) * 256 + (((lane >> 5) ^ (lane & 15)) * 16);
#pragma unroll
    for (int cb = 0; cb < 2; cb++)
#pragma unroll
      for (int rq = 0; rq < 4; rq++)
        *(LAS f32x4*)(smem + (wp ^ ((8 * cb + 2 * rq) * 16))) = (f32x4){o2[cb][4 * rq], o2[cb][4 * rq + 1], o2[cb][4 * rq + 2], o2[cb][4 * rq + 3]};
  }
  f32x4 u0 = {bf_lo(xs.x) + bf_lo(bv.x), bf_hi(xs.x) + bf_hi(bv.x), bf_lo(xs.y) + bf_lo(bv.y), bf_hi(xs.y) + bf_hi(bv.y)};
  f32x4 u1 = {bf_lo(xs.z) + bf_lo(bv.z), bf_hi(xs.z) + bf_hi(bv.z), bf_lo(xs.w) + bf_lo(bv.w), bf_hi(xs.w) + bf_hi(bv.w)};
  wait_lgkm0();
  bar();
  {
    const int row = wave * 8 + (lane >> 3);
    const unsigned rp = XCH + row * 256 + (((2 * (lane & 7)) ^ (row & 15)) * 16);
#pragma unroll
    for (int q = 0; q < 4; q++) { u0 += *(const LAS f32x4*)(smem + rp + q * 16384); u1 += *(const LAS f32x4*)(smem + (rp ^ 16) + q * 16384); }
    const u32x4 o = {pk_bf16(u0[0], u0[1]), pk_bf16(u0[2], u0[3]), pk_bf16(u1[0], u1[1]), pk_bf16(u1[2], u1[3])};
    __builtin_amdgcn_raw_buffer_store_b128(o, __builtin_amdgcn_make_buffer_rsrc(P.out, 0, 2048 * 1024, 0x00020000), orow * 1024 + ocol * 2, 0, 18);
  }
}

extern "C" void kernel_launch(void* const* d_in, const int* in_sizes, int n_in, void* d_out, int out_size, void* d_ws, size_t ws_size,
                              hipStream_t stream) {
  Params P;
  P.x = (const char*)d_in[0]; P.wqkv = (const char*)d_in[1]; P.wo = (const char*)d_in[2];
  P.w1 = (const char*)d_in[4]; P.b1 = (const char*)d_in[5]; P.w2 = (const char*)d_in[6]; P.b2 = (const char*)d_in[7];
  P.out = (char*)d_out; P.ws = (char*)d_ws;
  fused_block<<<256, 512, 0, stream>>>(P);
}
```
